```python
import torch

def _softmax(x, axis=-1):
    return torch.softmax(x, dim=axis)
def _gelu(x):
    return torch.nn.functional.gelu(x, approximate="tanh")
def _silu(x):
    return torch.nn.functional.silu(x)
def _sigmoid(x):
    return torch.sigmoid(x)
def _layernorm(x, axis=-1, eps=1e-5):
    m = x.mean(dim=axis, keepdim=True)
    v = x.var(dim=axis, keepdim=True, unbiased=False)
    return (x - m) / torch.sqrt(v + eps)
def _scatter_add(tgt, idx, src, axis=0):
    return tgt.clone().index_add_(axis, idx, src)
def _segment_sum(src, seg, n):
    out = torch.zeros((n,) + src.shape[1:], dtype=src.dtype)
    return out.index_add_(0, seg, src)
def _pad(x, pw):
    flat = tuple(v for pair in reversed(pw) for v in pair)
    return torch.nn.functional.pad(x, flat)

kernel_name = 'recurrent_scan__spectral_fnet__dense_activation_silu__prompt'

def setup_inputs():

    _w = (lambda a: (lambda t: t.to(torch.bfloat16) if t.is_floating_point() else t)(torch.as_tensor(a)))
    def _A_setup():
        B, S, D = 1, 2048, 640
        ns = torch.arange(S)[:, None] * torch.arange(S)[None, :]
        nd = torch.arange(D)[:, None] * torch.arange(D)[None, :]
        Ws_re = torch.cos(-2.0 * torch.pi * ns / S).to(float)
        Ws_im = torch.sin(-2.0 * torch.pi * ns / S).to(float)
        Wd_re = torch.cos(-2.0 * torch.pi * nd / D).to(float)
        Wd_im = torch.sin(-2.0 * torch.pi * nd / D).to(float)
        return {
            "x": _w((lambda *s: torch.randn(*s, dtype=torch.bfloat16) * 0.05)(B, S, D)),
            "dft_seq_re": _w(Ws_re),
            "dft_seq_im": _w(Ws_im),
            "dft_hid_re": _w(Wd_re),
            "dft_hid_im": _w(Wd_im),
        }
    _A_in = _A_setup()

    def _B_setup():
        B, S, D, F = 1, 2048, 640, 1536
        return {
            "x": _w((lambda *s: torch.randn(*s, dtype=torch.bfloat16) * 0.05)(B, S, D)),
            "w1": _w((lambda *s: torch.randn(*s, dtype=torch.bfloat16) * 0.2)(D, F) * (D ** -0.5)),
            "w2": _w((lambda *s: torch.randn(*s, dtype=torch.bfloat16) * 0.05)(F, D) * (F ** -0.5)),
        }
    _B_in = _B_setup()

    return {
        "A_x": _A_in["x"],
        "A_dft_seq_re": _A_in["dft_seq_re"],
        "A_dft_seq_im": _A_in["dft_seq_im"],
        "A_dft_hid_re": _A_in["dft_hid_re"],
        "A_dft_hid_im": _A_in["dft_hid_im"],
        "B_x": _B_in["x"],
        "B_w1": _B_in["w1"],
        "B_w2": _B_in["w2"],
    }

def reference(A_x, A_dft_seq_re, A_dft_seq_im, A_dft_hid_re, A_dft_hid_im, B_x, B_w1, B_w2):
    def _A_ref(x, dft_seq_re, dft_seq_im, dft_hid_re, dft_hid_im):
        a = torch.einsum("bsd,ed->bse", x, dft_hid_re)
        b = torch.einsum("bsd,ed->bse", x, dft_hid_im)
        y = torch.einsum("ts,bse->bte", dft_seq_re, a) - torch.einsum("ts,bse->bte", dft_seq_im, b)
        return y

    def _B_ref(x, w1, w2):
        h = torch.einsum("bsd,df->bsf", x, w1)
        h = _silu(h)
        out = torch.einsum("bsf,fd->bsd", h, w2)
        return out

    x = A_x
    xn = _layernorm(x, axis=-1)
    s = _A_ref(xn, A_dft_seq_re, A_dft_seq_im, A_dft_hid_re, A_dft_hid_im)
    g = _silu(_B_ref(xn, B_w1, B_w2))
    return x + s * g
```

```cpp
#include <hip/hip_runtime.h>
#include <hip/hip_bf16.h>
#include <stdint.h>

typedef unsigned short u16;
typedef short bf16x8 __attribute__((ext_vector_type(8)));
typedef float f32x16 __attribute__((ext_vector_type(16)));

static constexpr int S = 2048, D = 640, F = 1536;

__device__ __forceinline__ float bf2f(u16 v) { return __uint_as_float(((unsigned)v) << 16); }
__device__ __forceinline__ u16 f2bf(float f) {
    __hip_bfloat16 b = __float2bfloat16(f);
    return *reinterpret_cast<u16*>(&b);
}
__device__ __forceinline__ float silu(float v) { return v / (1.0f + __expf(-v)); }

__global__ __launch_bounds__(256) void k_ln(const u16* __restrict__ x, u16* __restrict__ xn) {
    int row = blockIdx.x * 4 + (threadIdx.x >> 6), lane = threadIdx.x & 63;
    const u16* p = x + (size_t)row * D;
    float v[10]; float s = 0;
    for (int i = 0; i < 10; ++i) { v[i] = bf2f(p[lane + 64 * i]); s += v[i]; }
    for (int o = 32; o; o >>= 1) s += __shfl_xor(s, o);
    float m = s * (1.0f / D), q = 0;
    for (int i = 0; i < 10; ++i) { float d = v[i] - m; q += d * d; }
    for (int o = 32; o; o >>= 1) q += __shfl_xor(q, o);
    float r = 1.0f / sqrtf(q * (1.0f / D) + 1e-5f);
    for (int i = 0; i < 10; ++i) xn[(size_t)row * D + lane + 64 * i] = f2bf((v[i] - m) * r);
}

template <bool BT>
__device__ __forceinline__ void gemm_tile(f32x16& acc, const u16* A, int lda, const u16* B, int ldb, int K, int row0, int col0, int lane, float sign) {
    int r = lane & 31, h = lane >> 5;
    for (int k = 0; k < K; k += 16) {
        bf16x8 a = *(const bf16x8*)(A + (size_t)(row0 + r) * lda + k + 8 * h);
        bf16x8 b;
        if (BT) b = *(const bf16x8*)(B + (size_t)(col0 + r) * ldb + k + 8 * h);
        else { for (int j = 0; j < 8; ++j) b[j] = (short)B[(size_t)(k + 8 * h + j) * ldb + col0 + r]; }
        if (sign < 0) for (int j = 0; j < 8; ++j) a[j] ^= (short)0x8000;
        acc = __builtin_amdgcn_mfma_f32_32x32x16_bf16(a, b, acc, 0, 0, 0);
    }
}

__global__ __launch_bounds__(64) void k_s1(const u16* xn, const u16* hr, const u16* hi, const u16* w1, u16* a, u16* b, u16* h) {
    int lane = threadIdx.x, row0 = blockIdx.y * 32, ct = blockIdx.x;
    f32x16 acc = {};
    u16* out; int ldo, col0;
    bool act = false;
    if (ct < 20) { col0 = ct * 32; gemm_tile<true>(acc, xn, D, hr, D, D, row0, col0, lane, 1.f); out = a; ldo = D; }
    else if (ct < 40) { col0 = (ct - 20) * 32; gemm_tile<true>(acc, xn, D, hi, D, D, row0, col0, lane, 1.f); out = b; ldo = D; }
    else { col0 = (ct - 40) * 32; gemm_tile<false>(acc, xn, D, w1, F, D, row0, col0, lane, 1.f); out = h; ldo = F; act = true; }
    for (int i = 0; i < 16; ++i) {
        int r = (i & 3) + 8 * (i >> 2) + 4 * (lane >> 5), c = lane & 31;
        float v = acc[i];
        if (act) { v = bf2f(f2bf(v)); v = silu(v); }
        out[(size_t)(row0 + r) * ldo + col0 + c] = f2bf(v);
    }
}

__global__ __launch_bounds__(64) void k_s2(const u16* x, const u16* cr, const u16* ci, const u16* a, const u16* b, const u16* h, const u16* w2, u16* out) {
    int lane = threadIdx.x, row0 = blockIdx.y * 32, col0 = blockIdx.x * 32;
    f32x16 y = {}, o = {};
    gemm_tile<false>(y, cr, S, a, D, S, row0, col0, lane, 1.f);
    gemm_tile<false>(y, ci, S, b, D, S, row0, col0, lane, -1.f);
    gemm_tile<false>(o, h, F, w2, D, F, row0, col0, lane, 1.f);
    for (int i = 0; i < 16; ++i) {
        int r = (i & 3) + 8 * (i >> 2) + 4 * (lane >> 5), c = lane & 31;
        size_t idx = (size_t)(row0 + r) * D + col0 + c;
        float g = silu(bf2f(f2bf(o[i])));
        out[idx] = f2bf(bf2f(x[idx]) + y[i] * g);
    }
}

extern "C" void kernel_launch(void* const* d_in, const int* in_sizes, int n_in,
                              void* d_out, int out_size, void* d_ws, size_t ws_size,
                              hipStream_t stream) {
    const u16* x = (const u16*)d_in[0];
    const u16* cr = (const u16*)d_in[1];
    const u16* ci = (const u16*)d_in[2];
    const u16* hr = (const u16*)d_in[3];
    const u16* hi = (const u16*)d_in[4];
    const u16* w1 = (const u16*)d_in[6];
    const u16* w2 = (const u16*)d_in[7];
    u16* ws = (u16*)d_ws;
    u16* xn = ws; u16* a = xn + S * D; u16* b = a + S * D; u16* h = b + S * D;
    k_ln<<<S / 4, 256, 0, stream>>>(x, xn);
    k_s1<<<dim3(88, S / 32), 64, 0, stream>>>(xn, hr, hi, w1, a, b, h);
    k_s2<<<dim3(D / 32, S / 32), 64, 0, stream>>>(x, cr, ci, a, b, h, w2, (u16*)d_out);
}
```

```cpp
#include <stdio.h>
#include <hip/hip_runtime.h>
#include <hip/hip_bf16.h>
#include <stdint.h>

typedef unsigned short u16;
typedef short bf16x8 __attribute__((ext_vector_type(8)));
typedef float f32x16 __attribute__((ext_vector_type(16)));

static constexpr int S = 2048, D = 640, F = 1536;

__device__ __forceinline__ float bf2f(u16 v) { return __uint_as_float(((unsigned)v) << 16); }
__device__ __forceinline__ u16 f2bf(float f) {
    __hip_bfloat16 b = __float2bfloat16(f);
    return *reinterpret_cast<u16*>(&b);
}
__device__ __forceinline__ float silu(float v) { return v / (1.0f + __expf(-v)); }

__global__ __launch_bounds__(256) void k_ln(const u16* __restrict__ x, u16* __restrict__ xn) {
    int row = blockIdx.x * 4 + (threadIdx.x >> 6), lane = threadIdx.x & 63;
    const u16* p = x + (size_t)row * D;
    float v[10]; float s = 0;
    for (int i = 0; i < 10; ++i) { v[i] = bf2f(p[lane + 64 * i]); s += v[i]; }
    for (int o = 32; o; o >>= 1) s += __shfl_xor(s, o);
    float m = s * (1.0f / D), q = 0;
    for (int i = 0; i < 10; ++i) { float d = v[i] - m; q += d * d; }
    for (int o = 32; o; o >>= 1) q += __shfl_xor(q, o);
    float r = 1.0f / sqrtf(q * (1.0f / D) + 1e-5f);
    for (int i = 0; i < 10; ++i) xn[(size_t)row * D + lane + 64 * i] = f2bf((v[i] - m) * r);
}

template <bool BT>
__device__ __forceinline__ void gemm_tile(f32x16& acc, const u16* A, int lda, const u16* B, int ldb, int K, int row0, int col0, int lane, float sign) {
    int r = lane & 31, h = lane >> 5;
    for (int k = 0; k < K; k += 16) {
        bf16x8 a = *(const bf16x8*)(A + (size_t)(row0 + r) * lda + k + 8 * h);
        bf16x8 b;
        if (BT) b = *(const bf16x8*)(B + (size_t)(col0 + r) * ldb + k + 8 * h);
        else { for (int j = 0; j < 8; ++j) b[j] = (short)B[(size_t)(k + 8 * h + j) * ldb + col0 + r]; }
        if (sign < 0) for (int j = 0; j < 8; ++j) a[j] ^= (short)0x8000;
        acc = __builtin_amdgcn_mfma_f32_32x32x16_bf16(a, b, acc, 0, 0, 0);
    }
}

__global__ __launch_bounds__(64) void k_s1(const u16* xn, const u16* hr, const u16* hi, const u16* w1, u16* a, u16* b, u16* h) {
    int lane = threadIdx.x, row0 = blockIdx.y * 32, ct = blockIdx.x;
    f32x16 acc = {};
    u16* out; int ldo, col0;
    bool act = false;
    if (ct < 20) { col0 = ct * 32; gemm_tile<true>(acc, xn, D, hr, D, D, row0, col0, lane, 1.f); out = a; ldo = D; }
    else if (ct < 40) { col0 = (ct - 20) * 32; gemm_tile<true>(acc, xn, D, hi, D, D, row0, col0, lane, 1.f); out = b; ldo = D; }
    else { col0 = (ct - 40) * 32; gemm_tile<false>(acc, xn, D, w1, F, D, row0, col0, lane, 1.f); out = h; ldo = F; act = true; }
    for (int i = 0; i < 16; ++i) {
        int r = (i & 3) + 8 * (i >> 2) + 4 * (lane >> 5), c = lane & 31;
        float v = acc[i];
        if (act) { v = bf2f(f2bf(v)); v = silu(v); }
        out[(size_t)(row0 + r) * ldo + col0 + c] = f2bf(v);
    }
}

__global__ __launch_bounds__(64) void k_s2(const u16* x, const u16* cr, const u16* ci, const u16* a, const u16* b, const u16* h, const u16* w2, u16* out) {
    int lane = threadIdx.x, row0 = blockIdx.y * 32, col0 = blockIdx.x * 32;
    f32x16 y = {}, o = {};
    gemm_tile<false>(y, cr, S, a, D, S, row0, col0, lane, 1.f);
    gemm_tile<false>(y, ci, S, b, D, S, row0, col0, lane, -1.f);
    gemm_tile<false>(o, h, F, w2, D, F, row0, col0, lane, 1.f);
    for (int i = 0; i < 16; ++i) {
        int r = (i & 3) + 8 * (i >> 2) + 4 * (lane >> 5), c = lane & 31;
        size_t idx = (size_t)(row0 + r) * D + col0 + c;
        float g = silu(bf2f(f2bf(o[i])));
        out[idx] = f2bf(bf2f(x[idx]) + y[i] * g);
    }
}

typedef unsigned long long u64;
struct P { const char* cr; const char* ci; const char* x; const char* w1; unsigned short* out; };
__global__ __launch_bounds__(512,2) void k_probe(P pp){
  u64 t0; asm volatile("s_memrealtime %0\n\ts_waitcnt lgkmcnt(0)":"=s"(t0));
  const char* kp=(const char*)__builtin_amdgcn_kernarg_segment_ptr();
  u64 pcr,pci,px,pw;
  asm volatile("s_load_dwordx2 %0, %4, 0\n\ts_load_dwordx2 %1, %4, 8\n\ts_load_dwordx2 %2, %4, 16\n\ts_load_dwordx2 %3, %4, 24\n\ts_waitcnt lgkmcnt(0)":"=&s"(pcr),"=&s"(pci),"=&s"(px),"=&s"(pw):"s"(kp));
  u64 t1; asm volatile("s_memrealtime %0\n\ts_waitcnt lgkmcnt(0)":"=s"(t1));
  const int tid=threadIdx.x, lane=tid&63; const int wid=__builtin_amdgcn_readfirstlane(tid>>6);
  const unsigned blk=blockIdx.x;
  unsigned v=0;
  u64 tA=0,tB=0,tC=0,tD=0;
  if(wid==0){
    unsigned off=(blk*80+lane)*128;
    asm volatile("s_nop 4\n\tglobal_load_dword %0, %1, %2\n\ts_waitcnt vmcnt(0)":"+v"(v):"v"(off),"s"(px):"memory");
  }
  asm volatile("s_memrealtime %0\n\ts_waitcnt lgkmcnt(0)":"=s"(tA));
  __syncthreads();
  u64 tb0; asm volatile("s_memrealtime %0\n\ts_waitcnt lgkmcnt(0)":"=s"(tb0));
  if(wid<4){
    unsigned off=(blk*256+wid*64+lane)*128;
    asm volatile("s_nop 4\n\tglobal_load_dword %0, %1, %2\n\ts_waitcnt vmcnt(0)":"+v"(v):"v"(off),"s"(pcr):"memory");
  }
  __syncthreads();
  asm volatile("s_memrealtime %0\n\ts_waitcnt lgkmcnt(0)":"=s"(tB));
  if(wid<4){
    unsigned off=(blk*256+wid*64+lane)*128;
    asm volatile("s_nop 4\n\tglobal_load_dword %0, %1, %2 nt\n\ts_waitcnt vmcnt(0)":"+v"(v):"v"(off),"s"(pci):"memory");
  }
  __syncthreads();
  asm volatile("s_memrealtime %0\n\ts_waitcnt lgkmcnt(0)":"=s"(tC));
  if(wid<4){
    unsigned off=(blk*256+wid*64+lane)*128+64;
    asm volatile("s_nop 4\n\tglobal_load_dword %0, %1, %2\n\ts_waitcnt vmcnt(0)":"+v"(v):"v"(off),"s"(pcr):"memory");
  }
  __syncthreads();
  asm volatile("s_memrealtime %0\n\ts_waitcnt lgkmcnt(0)":"=s"(tD));
  if(tid==0 && (blk==0||blk==131||blk==255)) printf("blk %u start %llu: kernarg %llu, x(80 lines) %llu, Cr(def) %llu, Ci(nt) %llu, Cr again %llu  (x10ns) v=%u\n",blk,t0%1000000,t1-t0,tA-t1,tB-tb0,tC-tB,tD-tC,v&1);
  if(v==0x12345678) pp.out[tid]=1;
}
extern "C" void kernel_launch(void* const* d_in, const int* in_sizes, int n_in,
                              void* d_out, int out_size, void* d_ws, size_t ws_size,
                              hipStream_t stream) {
    const u16* x = (const u16*)d_in[0];
    const u16* cr = (const u16*)d_in[1];
    const u16* ci = (const u16*)d_in[2];
    const u16* hr = (const u16*)d_in[3];
    const u16* hi = (const u16*)d_in[4];
    const u16* w1 = (const u16*)d_in[6];
    const u16* w2 = (const u16*)d_in[7];
    u16* ws = (u16*)d_ws;
    u16* xn = ws; u16* a = xn + S * D; u16* b = a + S * D; u16* h = b + S * D;
    { P p{(const char*)d_in[1],(const char*)d_in[2],(const char*)d_in[0],(const char*)d_in[6],(unsigned short*)d_out}; k_probe<<<256,512,0,stream>>>(p); }
    k_ln<<<S / 4, 256, 0, stream>>>(x, xn);
    k_s1<<<dim3(88, S / 32), 64, 0, stream>>>(xn, hr, hi, w1, a, b, h);
    k_s2<<<dim3(D / 32, S / 32), 64, 0, stream>>>(x, cr, ci, a, b, h, w2, (u16*)d_out);
}
```

```cpp
#include <hip/hip_runtime.h>
#include <stdint.h>
#include <utility>

#define DI __device__ __forceinline__
typedef short bf16x8 __attribute__((ext_vector_type(8)));
typedef short bf16x4 __attribute__((ext_vector_type(4)));
typedef float f32x4 __attribute__((ext_vector_type(4)));
typedef float f32x2 __attribute__((ext_vector_type(2)));
typedef unsigned u32x4 __attribute__((ext_vector_type(4)));
typedef unsigned u32x2 __attribute__((ext_vector_type(2)));
typedef unsigned long long u64;
template <int... I> using iseq = std::integer_sequence<int, I...>;
template <int N> using mkseq = std::make_integer_sequence<int, N>;

struct Params {
  const char* x; const char* cr; const char* ci; const char* hr; const char* hi;
  const char* w1; const char* w2; char* out; char* ws;
};

constexpr unsigned WS_AB = 0;
constexpr unsigned WS_H  = WS_AB + 5242880;
constexpr unsigned WS_W2 = WS_H + 6291456;
constexpr unsigned WS_YP = WS_W2 + 1966080;
constexpr unsigned WS_FA = WS_YP + 10485760;
constexpr unsigned WS_FB = WS_FA + 1024;
constexpr unsigned WS_FC = WS_FB + 1024;
constexpr unsigned WS_END = WS_FC + 1024;

constexpr unsigned L_XN = 0;
constexpr unsigned L_R1 = 81920;
constexpr unsigned L_W2S = 155648;
constexpr unsigned L_BR = 0;
constexpr unsigned L_AR = 81920;
constexpr unsigned L_SIZE = 163840;

#define EP0 1
#define EPN 6
#define SPIN_MAX 2000000

template <int OFF> DI void dsr(bf16x8& d, unsigned a) { static_assert(OFF >= 0 && OFF < 65536, ""); asm volatile("ds_read_b128 %0, %1 offset:%2" : "=v"(d) : "v"(a), "i"(OFF)); }
template <int OFF> DI void dstr(bf16x4& d, unsigned a) { static_assert(OFF >= 0 && OFF < 65536, ""); asm volatile("ds_read_b64_tr_b16 %0, %1 offset:%2" : "=v"(d) : "v"(a), "i"(OFF)); }
template <int OFF> DI void dsw(unsigned a, const u32x4& d) { static_assert(OFF >= 0 && OFF < 65536, ""); asm volatile("ds_write_b128 %0, %1 offset:%2" ::"v"(a), "v"(d), "i"(OFF) : "memory"); }
template <bool Z> DI void mfma(f32x4& c, const bf16x8& a, const bf16x8& b) {
  if constexpr (Z) asm volatile("v_mfma_f32_16x16x32_bf16 %0, %1, %2, 0" : "=&v"(c) : "v"(a), "v"(b));
  else asm volatile("v_mfma_f32_16x16x32_bf16 %0, %1, %2, %0" : "+v"(c) : "v"(a), "v"(b));
}
template <int N> DI void waitl() { static_assert(N >= 0 && N < 16, ""); __builtin_amdgcn_s_waitcnt(0xC07F | (N << 8)); }
template <int N> DI void waitv() { static_assert(N >= 0 && N < 64, ""); asm volatile("s_waitcnt vmcnt(%0)" ::"i"(N) : "memory"); }
DI void bar() { asm volatile("s_barrier" ::: "memory"); }
DI void mfma_settle() { asm volatile("s_nop 7\n\ts_nop 7" ::: "memory"); }

#define DMA4(POL)                                                                                         \
  asm volatile("s_mov_b32 m0, %0\n\ts_nop 0\n\t"                                                          \
               "global_load_lds_dwordx4 %1, %5 " POL "\n\t"                                               \
               "global_load_lds_dwordx4 %2, %5 offset:1024 " POL "\n\t"                                   \
               "global_load_lds_dwordx4 %3, %5 offset:2048 " POL "\n\t"                                   \
               "global_load_lds_dwordx4 %4, %5 offset:3072 " POL ::"s"(m), "v"(v0), "v"(v1), "v"(v2), "v"(v3), "s"(s) : "memory")
template <bool SC1> DI void dma4(unsigned m, unsigned v0, unsigned v1, unsigned v2, unsigned v3, const char* s) {
  if constexpr (SC1) DMA4("sc1"); else DMA4("");
}
#define DMA2(POL)                                                                                         \
  asm volatile("s_mov_b32 m0, %0\n\ts_nop 0\n\t"                                                          \
               "global_load_lds_dwordx4 %1, %3 " POL "\n\t"                                               \
               "global_load_lds_dwordx4 %2, %3 offset:1024 " POL ::"s"(m), "v"(v0), "v"(v1), "s"(s) : "memory")
template <bool SC1> DI void dma2(unsigned m, unsigned v0, unsigned v1, const char* s) {
  if constexpr (SC1) DMA2("sc1"); else DMA2("");
}
#define DMA1(POL)                                                                                         \
  asm volatile("s_mov_b32 m0, %0\n\ts_nop 0\n\t"                                                          \
               "global_load_lds_dwordx4 %1, %2 " POL ::"s"(m), "v"(v0), "s"(s) : "memory")
template <bool SC1> DI void dma1(unsigned m, unsigned v0, const char* s) {
  if constexpr (SC1) DMA1("sc1"); else DMA1("");
}

template <int OFF> DI void gst16(const char* base, unsigned voff, const u32x4& d) {
  asm volatile("global_store_dwordx4 %0, %1, %2 offset:%3 sc1\n\ts_nop 1" ::"v"(voff), "v"(d), "s"(base), "i"(OFF) : "memory");
}
DI void gst4(const char* base, unsigned voff, unsigned d) {
  asm volatile("global_store_dword %0, %1, %2 sc1" ::"v"(voff), "v"(d), "s"(base) : "memory");
}
DI unsigned pk2(float lo, float hi) { unsigned r; asm("v_cvt_pk_bf16_f32 %0, %1, %2" : "=v"(r) : "v"(lo), "v"(hi)); return r; }
DI float bflo(unsigned v) { return __uint_as_float(v << 16); }
DI float bfhi(unsigned v) { return __uint_as_float(v & 0xffff0000u); }
DI float silu(float v) { return v * __builtin_amdgcn_rcpf(1.0f + __expf(-v)); }
template <int CTRL> DI float dppadd(float v) {
  return v + __int_as_float(__builtin_amdgcn_update_dpp(0, __float_as_int(v), CTRL, 0xf, 0xf, true));
}
DI float red8(float v) { v = dppadd<0xB1>(v); v = dppadd<0x4E>(v); return dppadd<0x141>(v); }

DI void touch(unsigned& d, const char* a) { asm volatile("global_load_dword %0, %1, off" : "+v"(d) : "v"(a) : "memory"); }
DI void touchs(unsigned& d, unsigned voff, const char* base) { asm volatile("global_load_dword %0, %1, %2" : "+v"(d) : "v"(voff), "s"(base) : "memory"); }

DI void touch2_if(unsigned& d, int n, int ep, unsigned voff, const char* b0, const char* b1) {
  asm volatile("s_cmp_lg_u32 %1, %2\n\ts_cbranch_scc1 .Lskip_touch%=\n\t"
               "global_load_dword %0, %3, %4\n\tglobal_load_dword %0, %3, %5\n"
               ".Lskip_touch%=:" : "+v"(d) : "s"(n), "s"(ep), "v"(voff), "s"(b0), "s"(b1) : "memory", "scc");
}

DI void flag_wait(unsigned& fv, const char* base, unsigned voff, unsigned tok) {
  asm volatile("s_waitcnt vmcnt(0)" : "+v"(fv)::"memory");
  for (int n = 0; __builtin_amdgcn_ballot_w64(fv != tok) != 0 && n < SPIN_MAX; ++n)
    asm volatile("global_load_dword %0, %1, %2 sc1\n\ts_waitcnt vmcnt(0)" : "=v"(fv) : "v"(voff), "s"(base) : "memory");
}
DI void flag_req(unsigned& fv, const char* base, unsigned voff) {
  asm volatile("global_load_dword %0, %1, %2 sc1" : "+v"(fv) : "v"(voff), "s"(base) : "memory");
}
DI void flag_wait4(u32x4& fv, const char* base, unsigned voff, unsigned tok) {
  asm volatile("s_waitcnt vmcnt(0)" : "+v"(fv)::"memory");
  for (int n = 0; __builtin_amdgcn_ballot_w64(fv[0] != tok || fv[1] != tok || fv[2] != tok || fv[3] != tok) != 0 && n < SPIN_MAX; ++n)
    asm volatile("global_load_dwordx4 %0, %1, %2 sc1\n\ts_waitcnt vmcnt(0)" : "=v"(fv) : "v"(voff), "s"(base) : "memory");
}
DI void flag_req4(u32x4& fv, const char* base, unsigned voff) {
  asm volatile("global_load_dwordx4 %0, %1, %2 sc1" : "+v"(fv) : "v"(voff), "s"(base) : "memory");
}

#define STAMP(k) do {} while (0)
#define LT() do {} while (0)

struct Ctx {
  const char* ws; unsigned tok; int cw; int lane;
};

struct FrA { bf16x8 a[2], b[5]; };
struct AdA { unsigned a0, a1, a0h, a1h, b0, b1, b0s, b1s; };
template <int I, int T, int KS> DI void rdA(FrA& f, const AdA& ad) {
  if constexpr (I < 2) {
    constexpr int off = (T & 7) * 8192 + I * 512;
    dsr<off>(f.a[I], T < 8 ? (KS ? ad.a1 : ad.a0) : (KS ? ad.a1h : ad.a0h));
  } else {
    constexpr int n = I - 2, s = T & 3;
    if constexpr (s < 3) dsr<s * 20480 + n * 2048>(f.b[n], KS ? ad.b1 : ad.b0);
    else dsr<n * 2048>(f.b[n], KS ? ad.b1s : ad.b0s);
  }
}
template <int G, int T, int KS, bool RD> DI void gapA(FrA& y, const AdA& ad) {
  if constexpr (RD && G < 4) { rdA<2 * G, T, KS>(y, ad); if constexpr (2 * G + 1 < 7) rdA<2 * G + 1, T, KS>(y, ad); }
}
template <int T, int KS, bool RD, bool Z, int... G> DI void halfA(f32x4 (&acc)[2][5], FrA& x, FrA& y, const AdA& ad, iseq<G...>) {
  ((mfma<Z>(acc[G / 5][G % 5], x.a[G / 5], x.b[G % 5]), gapA<G, T, KS, RD>(y, ad)), ...);
}
template <int T> DI void tileA(f32x4 (&acc)[2][5], FrA& x, FrA& y, const AdA& ad) {
  halfA<T, 1, true, T == 0>(acc, x, y, ad, mkseq<10>{});
  waitl<0>(); bar();
  halfA<T + 1, 0, (T < 9), false>(acc, y, x, ad, mkseq<10>{});
  if constexpr (T < 9) waitl<0>();
}
template <int... T> DI void stageA(f32x4 (&acc)[2][5], FrA& x, FrA& y, const AdA& ad, iseq<T...>) { (tileA<T>(acc, x, y, ad), ...); }
template <int... I> DI void rdA_first(FrA& x, const AdA& ad, iseq<I...>) { (rdA<I, 0, 0>(x, ad), ...); }

struct FrB { bf16x8 a[2]; bf16x4 w[6][2]; };
struct AdB { unsigned w[4]; };
template <int I, int U, int KS> DI void rdB(FrB& f, const AdA& aa, const AdB& ad) {
  if constexpr (I < 2) {
    constexpr int off = (U & 7) * 8192 + I * 512;
    dsr<off>(f.a[I], U < 8 ? (KS ? aa.a1 : aa.a0) : (KS ? aa.a1h : aa.a0h));
  } else {
    constexpr int n = (I - 2) >> 1, h = (I - 2) & 1;
    constexpr int off = ((U + 2) % 3) * 24576 + KS * 12288 + h * 1536 + (n >> 2) * 128;
    dstr<off>(f.w[n][h], ad.w[n & 3]);
  }
}
template <int G, int U, int KS, bool RD> DI void gapB(FrB& y, const AdA& aa, const AdB& ad) {
  if constexpr (RD && G < 7) { rdB<2 * G, U, KS>(y, aa, ad); rdB<2 * G + 1, U, KS>(y, aa, ad); }
}
DI bf16x8 cat(const bf16x4& l, const bf16x4& h) { return __builtin_shufflevector(l, h, 0, 1, 2, 3, 4, 5, 6, 7); }
template <int U, int KS, bool RD, bool Z, int... G> DI void halfB(f32x4 (&acc)[2][6], FrB& x, FrB& y, const AdA& aa, const AdB& ad, iseq<G...>) {
  ((mfma<Z>(acc[G / 6][G % 6], cat(x.w[G % 6][0], x.w[G % 6][1]), x.a[G / 6]), gapB<G, U, KS, RD>(y, aa, ad)), ...);
}
template <int... I> DI void rdB_first(FrB& x, const AdA& aa, const AdB& ad, iseq<I...>) { (rdB<I, 0, 0>(x, aa, ad), ...); }

struct FrC { bf16x8 a[4], b[5]; };
struct AdC { unsigned a0, a1, b0, b1; };
template <int I, int S, int KS> DI void rdC(FrC& f, const AdC& ad) {
  if constexpr (I >= 1 && I <= 5) {
    constexpr int n = I - 1, off = S * 20480 + KS * 10240 + n * 1024;
    if constexpr (S < 3) dsr<off>(f.b[n], ad.b0); else dsr<off - 3 * 20480>(f.b[n], ad.b1);
  } else {
    constexpr int m = (I == 0) ? 0 : I - 5, off = S * 16384 + (m >> 1) * 4096 + (m & 1) * 512;
    dsr<off>(f.a[m], KS ? ad.a1 : ad.a0);
  }
}
template <int G, int S> DI void gapC0(FrC& y, const AdC& ad) {
  if constexpr (G < 9) rdC<G, S, 1>(y, ad);
  if constexpr (G == 4) waitl<7>();
  if constexpr (G == 9) waitl<10>();
  if constexpr (G == 14) waitl<9>();
}
template <int G, int S> DI void gapC1(FrC& x, const AdC& ad) {
  if constexpr (G < 9) rdC<G, (S + 1) & 3, 0>(x, ad);
  if constexpr (G == 19) waitl<3>();
}
template <int S, int... G> DI void halfC0(f32x4 (&acc)[4][5], FrC& x, FrC& y, const AdC& ad, iseq<G...>) {
  ((mfma<false>(acc[G / 5][G % 5], x.b[G % 5], x.a[G / 5]), gapC0<G, S>(y, ad)), ...);
}
template <int S, int... G> DI void halfC1(f32x4 (&acc)[4][5], FrC& x, FrC& y, const AdC& ad, iseq<G...>) {
  ((mfma<false>(acc[G / 5][G % 5], y.b[G % 5], y.a[G / 5]), gapC1<G, S>(x, ad)), ...);
}
template <int... I> DI void rdC_first(FrC& x, const AdC& ad, iseq<I...>) { (rdC<I, 0, 0>(x, ad), ...); }

template <int NN> struct FrD { bf16x8 h[2], w[NN]; };
struct AdD { unsigned a, b, bs; };
template <int NN, int I, int S, int KS> DI void rdD(FrD<NN>& f, const AdD& ad) {
  if constexpr (I < 2) dsr<S * 16384 + KS * 4096 + I * 1024>(f.h[I], ad.a);
  else if constexpr (S < 3) dsr<S * 20480 + KS * 5120 + (I - 2) * 1024>(f.w[I - 2], ad.b);
  else dsr<KS * 5120 + (I - 2) * 1024>(f.w[I - 2], ad.bs);
}
template <int NN, int G, int S, int KS> DI void gapD(FrD<NN>& y, const AdD& ad) {
  if constexpr (2 * G < NN + 2) rdD<NN, 2 * G, S, KS>(y, ad);
  if constexpr (2 * G + 1 < NN + 2) rdD<NN, 2 * G + 1, S, KS>(y, ad);
}
template <int NN, int S, int KS, int... G> DI void stepD(f32x4 (&acc)[2][NN], FrD<NN>& x, FrD<NN>& y, const AdD& ad, iseq<G...>) {
  ((mfma<false>(acc[G / NN][G % NN], x.w[G % NN], x.h[G / NN]), gapD<NN, G, S, KS>(y, ad)), ...);
}
template <int NN, int S> DI void tileD(f32x4 (&acc)[2][NN], FrD<NN>& x, FrD<NN>& y, const AdD& ad) {
  stepD<NN, S, 1>(acc, x, y, ad, mkseq<2 * NN>{}); waitl<0>();
  stepD<NN, S, 2>(acc, y, x, ad, mkseq<2 * NN>{}); waitl<0>();
  stepD<NN, S, 3>(acc, x, y, ad, mkseq<2 * NN>{}); waitl<0>();
}
template <int NN, int... I> DI void rdD_first(FrD<NN>& x, const AdD& ad, iseq<I...>) { (rdD<NN, I, 0, 0>(x, ad), ...); }

template <int NN>
DI void stage3(const Params& p, int cw, int lane, int blk, int wid, unsigned tok, int rb2, int cb2, int j) {
  const int wm3 = cw >> 1;
  f32x4 acc[2][NN];
#pragma unroll
  for (int m = 0; m < 2; ++m)
#pragma unroll
    for (int n = 0; n < NN; ++n) { acc[m][n] = f32x4{0, 0, 0, 0}; asm volatile("" : "+v"(acc[m][n])); }
  AdD ad;
  ad.a = L_AR + wm3 * 2048 + lane * 16;
  ad.b = L_BR + (NN == 3 ? 0 : 3072) + lane * 16;
  ad.bs = ad.b + 3 * 20480;
  asm volatile("" : "+v"(ad.a), "+v"(ad.b), "+v"(ad.bs));
  FrD<NN> x, y;
  rdD_first<NN>(x, ad, mkseq<NN + 2>{});
  waitl<0>();
  const int tile = rb2 * 4 + cb2;
  unsigned fv = 0;
  const unsigned fco = WS_FC + (tile * 4 + (lane & 3)) * 4;
  u32x4 yp[4][NN];
  u32x2 xv[2][NN];
  const int i = lane & 15, q = lane >> 4;
  const unsigned yoff = WS_YP + ((tile * 4 + j) * 4) * 10240 + (wm3 * 5 + (NN == 3 ? 0 : 3)) * 1024 + lane * 16;
  const unsigned trow = rb2 * 128 + 64 * (j >> 1) + 32 * wm3 + 8 * (i >> 2) + (i & 3);
  const unsigned xoff = trow * 1280 + (cb2 * 160 + 80 * (j & 1) + (NN == 3 ? 0 : 48) + 4 * q) * 2;
  auto group = [&](auto LAST, int g) {
    constexpr bool last = LAST.value;
    tileD<NN, 0>(acc, x, y, ad);
    if (!last && g == 1) waitv<0>();
    bar();
    if (!last && g == 1 && cw == 0 && lane == 0) gst4(p.ws, WS_FC + (tile * 4 + j) * 4, tok);
    stepD<NN, 1, 0>(acc, y, x, ad, mkseq<2 * NN>{}); waitl<0>();
    tileD<NN, 1>(acc, x, y, ad);
    if constexpr (last) { if (cw == 0) flag_wait(fv, p.ws, fco, tok); }
    bar();
    if constexpr (last) {
#pragma unroll
      for (int jj = 0; jj < 4; ++jj)
#pragma unroll
        for (int n = 0; n < NN; ++n)
          asm volatile("global_load_dwordx4 %0, %1, %2 sc1" : "=v"(yp[jj][n]) : "v"(yoff + jj * 10240 + n * 1024), "s"(p.ws) : "memory");
#pragma unroll
      for (int m = 0; m < 2; ++m)
#pragma unroll
        for (int n = 0; n < NN; ++n)
          asm volatile("global_load_dwordx2 %0, %1, %2" : "=v"(xv[m][n]) : "v"(xoff + m * 4 * 1280 + n * 32), "s"(p.x) : "memory");
    }
    stepD<NN, 2, 0>(acc, y, x, ad, mkseq<2 * NN>{}); waitl<0>();
    tileD<NN, 2>(acc, x, y, ad);
    bar();
    if (!last && g == 1 && cw == 0) flag_req(fv, p.ws, fco);
    stepD<NN, 3, 0>(acc, y, x, ad, mkseq<2 * NN>{}); waitl<0>();
    tileD<NN, 3>(acc, x, y, ad);
    bar();
    stepD<NN, 0, 0>(acc, y, x, ad, mkseq<2 * NN>{}); waitl<0>();
  };
#pragma unroll 1
  for (int g = 0; g < 2; ++g) group(std::false_type{}, g);
  group(std::true_type{}, 2);
  STAMP(9);
  mfma_settle();
#pragma unroll
  for (int jj = 0; jj < 4; ++jj)
#pragma unroll
    for (int n = 0; n < NN; ++n) asm volatile("s_waitcnt vmcnt(0)" : "+v"(yp[jj][n])::"memory");
#pragma unroll
  for (int m = 0; m < 2; ++m)
#pragma unroll
    for (int n = 0; n < NN; ++n) asm volatile("" : "+v"(xv[m][n]));
#pragma unroll
  for (int m = 0; m < 2; ++m)
#pragma unroll
    for (int n = 0; n < NN; ++n) {
      float r[4];
#pragma unroll
      for (int e = 0; e < 4; ++e) {
        float y4 = 0.f;
#pragma unroll
        for (int jj = 0; jj < 4; ++jj) { unsigned w = yp[jj][n][m * 2 + (e >> 1)]; y4 += (e & 1) ? bfhi(w) : bflo(w); }
        unsigned xw = xv[m][n][e >> 1];
        float xx = (e & 1) ? bfhi(xw) : bflo(xw);
        r[e] = xx + y4 * silu(acc[m][n][e]);
      }
      u32x2 o = {pk2(r[0], r[1]), pk2(r[2], r[3])};
      asm volatile("global_store_dwordx2 %0, %1, %2" ::"v"(xoff + m * 4 * 1280 + n * 32), "v"(o), "s"(p.out) : "memory");
    }
}

__global__ __launch_bounds__(512, 2) void fnet_fused(Params p) {
  __shared__ __attribute__((aligned(16))) char lds[L_SIZE];
  const int tid = threadIdx.x, lane = tid & 63;
  const int wid = __builtin_amdgcn_readfirstlane(tid >> 6);
  const int blk = blockIdx.x, xcd = blk & 7, idx = blk >> 3;
  const int rb1 = (xcd & 1) * 16 + (idx >> 1), cb1 = (xcd >> 1) * 2 + (idx & 1);
  const int j = xcd & 3, rb2 = (xcd >> 2) * 8 + (idx >> 2), cb2 = idx & 3;
  const unsigned tok = 1;
  asm volatile("" ::"v"((unsigned)(size_t)lds));
  STAMP(0);

  u32x4 xr[10];
  auto xload = [&]() {
    const unsigned xo = (rb1 * 64 + wid * 8 + (lane >> 3)) * 1280 + (lane & 7) * 16;
    asm volatile(
        "global_load_dwordx4 %0, %10, %11\n\tglobal_load_dwordx4 %1, %10, %11 offset:128\n\t"
        "global_load_dwordx4 %2, %10, %11 offset:256\n\tglobal_load_dwordx4 %3, %10, %11 offset:384\n\t"
        "global_load_dwordx4 %4, %10, %11 offset:512\n\tglobal_load_dwordx4 %5, %10, %11 offset:640\n\t"
        "global_load_dwordx4 %6, %10, %11 offset:768\n\tglobal_load_dwordx4 %7, %10, %11 offset:896\n\t"
        "global_load_dwordx4 %8, %10, %11 offset:1024\n\tglobal_load_dwordx4 %9, %10, %11 offset:1152"
        : "=&v"(xr[0]), "=&v"(xr[1]), "=&v"(xr[2]), "=&v"(xr[3]), "=&v"(xr[4]), "=&v"(xr[5]), "=&v"(xr[6]), "=&v"(xr[7]), "=&v"(xr[8]), "=&v"(xr[9])
        : "v"(xo), "s"(p.x) : "memory");
  };
  auto htouch = [&](unsigned& dummy, int w) {
    const char* hb = ((xcd >> 1) < 2 ? p.hr : p.hi) + (size_t)((xcd >> 1) & 1) * 320 * 1280;
    touchs(dummy, (idx * 100 + w * 25 + (lane < 25 ? lane : 24)) * 128, hb);
  };
  auto wtouch = [&](unsigned& dummy, int w) {
    const int n = idx * 120 + w * 30 + (lane < 30 ? lane : 29);
    const int l2 = lane < 48 ? lane : 47;
    const int pb = blk < 240 ? blk : 0, r = w * 8 + ((l2 - 32) >> 1);
    const char* a = lane < 32 ? p.w1 + (size_t)(n / 6) * 3072 + (xcd >> 1) * 768 + (n % 6) * 128
                              : p.w2 + (size_t)((pb / 5) * 32 + r) * 1280 + (pb % 5) * 256 + (l2 & 1) * 128;
    touch(dummy, a);
  };
  auto layernorm = [&]() {
    f32x2 v[40];
    f32x2 s = {0.f, 0.f};
#pragma unroll
    for (int k = 0; k < 10; ++k)
#pragma unroll
      for (int e = 0; e < 4; ++e) { v[k * 4 + e] = f32x2{bflo(xr[k][e]), bfhi(xr[k][e])}; s += v[k * 4 + e]; }
    const float mean = red8(s[0] + s[1]) * (1.0f / 640.0f);
    f32x2 s2 = {0.f, 0.f};
#pragma unroll
    for (int k = 0; k < 40; ++k) { v[k] -= f32x2{mean, mean}; s2 += v[k] * v[k]; }
    const float rstd = __builtin_amdgcn_rsqf(red8(s2[0] + s2[1]) * (1.0f / 640.0f) + 1e-5f);
    const int row = wid * 8 + (lane >> 3);
    const int key = ((row >> 1) & 1) | (((row >> 3) & 3) << 1);
    const unsigned a = L_XN + row * 128 + (((lane & 7) ^ key) << 4);
    const unsigned ah = a + 65536;
    auto put = [&](auto K) {
      constexpr int k = K.value;
      u32x4 o;
#pragma unroll
      for (int e = 0; e < 4; ++e) { f32x2 t = v[k * 4 + e] * f32x2{rstd, rstd}; o[e] = pk2(t[0], t[1]); }
      if constexpr (k < 8) dsw<k * 8192>(a, o); else dsw<(k - 8) * 8192>(ah, o);
    };
    [&]<int... K>(iseq<K...>) { (put(std::integral_constant<int, K>{}), ...); }(mkseq<10>{});
    waitl<0>();
  };

  if (wid >= 4) {
    const int lw = wid - 4;
    xload();
    unsigned va[5];
#pragma unroll
    for (int u = 0; u < 5; ++u) {
      const int row = lw * 40 + u * 8 + (lane >> 3);
      va[u] = row * 1280 + (((lane & 7) ^ ((row >> 1) & 7)) << 4) - (u < 4 ? u * 1024 : 0);
    }
    const char* ha = (cb1 < 4 ? p.hr : p.hi) + (size_t)(cb1 & 3) * 160 * 1280;
    auto issA = [&](int t) {
      const unsigned m = L_R1 + (t & 3) * 20480 + lw * 5120;
      dma4<false>(m, va[0], va[1], va[2], va[3], ha + t * 128);
      dma1<false>(m + 4096, va[4], ha + t * 128);
    };
    unsigned dummy = 0;
    asm volatile("s_waitcnt vmcnt(0)" : "+v"(xr[0]), "+v"(xr[1]), "+v"(xr[2]), "+v"(xr[3]), "+v"(xr[4]), "+v"(xr[5]), "+v"(xr[6]), "+v"(xr[7]), "+v"(xr[8]), "+v"(xr[9])::"memory");
    STAMP(1);
    issA(0); issA(1); issA(2); issA(3);
    wtouch(dummy, lw);
    const int ep = EP0 + (((idx >> 1) * EPN) >> 2);
    const char* tb = (j < 2 ? p.cr : p.ci) + (size_t)((xcd >> 2) * 1024 + (idx & 1) * 512 + lw * 128) * 4096 + (j & 1) * 2048 + (idx >> 1) * 128;
    const unsigned tvo = lane * 4096;
    auto episode = [&](int n) {
      touch2_if(dummy, n, ep, tvo, tb, tb + 64 * 4096);
    };
    layernorm();
    unsigned vb[6];
#pragma unroll
    for (int u = 0; u < 6; ++u) {
      const int G = u * 64 + lane, row = lw * 16 + G / 24, c = G % 24;
      const int g = ((row >> 1) & 1) | (((row >> 3) & 1) << 1);
      vb[u] = row * 3072 + ((c ^ (g << 1)) << 4) - (u < 4 ? u * 1024 : (u - 4) * 1024);
    }
    const char* wb = p.w1 + cb1 * 384;
    auto issB = [&](int u) {
      const unsigned m = L_R1 + ((u + 2) % 3) * 24576 + lw * 6144;
      const char* s = wb + (size_t)u * (64 * 3072);
      dma4<false>(m, vb[0], vb[1], vb[2], vb[3], s);
      dma2<false>(m + 4096, vb[4], vb[5], s);
    };
    waitv<15>();
    bar(); episode(1); LT();
    waitv<10>(); bar(); issA(4); episode(2); LT();
    waitv<10>(); bar(); issA(5); episode(3); LT();
    waitv<10>(); bar(); issA(6); episode(4); LT();
    waitv<10>(); bar(); issA(7); episode(5); LT();
    waitv<10>(); bar(); issA(8); episode(6); LT();
    waitv<10>(); bar(); issA(9); episode(7); LT();
    waitv<10>(); bar(); episode(8); LT();
    waitv<5>();  bar(); issB(0); episode(9); LT();
    waitv<6>();  bar(); episode(10); LT();
    waitv<0>();  bar(); issB(1); issB(2); episode(11); LT();
    unsigned vc[4];
#pragma unroll
    for (int u = 0; u < 4; ++u) {
      const int row = 8 * u + (lane >> 3), key = ((row >> 1) & 1) | (u << 1);
      vc[u] = row * 4096 + (((lane & 7) ^ key) << 4) - u * 1024;
    }
    const char* ca = (j < 2 ? p.cr : p.ci) + (size_t)(rb2 * 128 + lw * 32) * 4096 + (j & 1) * 2048;
    const unsigned vl = lane * 16;
    const char* cb = p.ws + WS_AB + (size_t)(j * 32 * 40 + cb2 * 10) * 1024 + (lw >> 1) * 40960 + (lw & 1) * 5120;
    auto issCA = [&](int t) { dma4<false>(L_AR + (t & 3) * 16384 + lw * 4096, vc[0], vc[1], vc[2], vc[3], ca + t * 128); };
    auto issCB = [&](int t) {
      const unsigned m = L_BR + (t & 3) * 20480 + lw * 5120;
      const char* s = cb + (size_t)t * 81920;
      dma4<true>(m, vl, vl, vl, vl, s);
      dma1<true>(m + 4096, vl, s + 4096);
    };
    waitv<6>(); bar(); issB(3); episode(12); LT();
    waitv<6>(); bar(); issB(4); episode(13); LT();
    waitv<6>(); bar(); issB(5); episode(14); LT();
    waitv<6>(); bar(); issB(6); episode(15); LT();
    waitv<6>(); bar(); issB(7); episode(16); LT();
    if (blk < 240) {
      const unsigned vw = (lw * 8 + (lane >> 4)) * 1280 + (lane & 15) * 16;
      dma2<false>(L_W2S + lw * 2048, vw, vw + 4 * 1280 - 1024, p.w2 + (size_t)(blk / 5) * 32 * 1280 + (blk % 5) * 256);
    }
    waitv<6>(); bar(); issB(8); episode(17); LT();
    waitv<6>(); bar(); issB(9); episode(18); LT();
    waitv<6>(); bar(); issCA(0); episode(19); LT();
    waitv<4>(); bar(); issCA(1); issCA(2); issCB(0); issCB(1); issCB(2); episode(20); LT();
    waitv<10>(); bar(); issCA(3); issCB(3); episode(21); LT();
    const char* da = p.ws + WS_H + (size_t)(rb2 * 2 + (j >> 1)) * 48 * 4096 + lw * 4096;
    const char* db = p.ws + WS_W2 + (size_t)(cb2 * 2 + (j & 1)) * 48 * 5120 + lw * 5120;
    auto issD = [&](int d) {
      dma4<true>(L_AR + (d & 3) * 16384 + lw * 4096, vl, vl, vl, vl, da + (size_t)d * 16384);
      const unsigned m = L_BR + (d & 3) * 20480 + lw * 5120;
      const char* s = db + (size_t)d * 20480;
      dma4<true>(m, vl, vl, vl, vl, s);
      dma1<true>(m + 4096, vl, s + 4096);
    };
    waitv<14>(); bar(); issCA(4); issCB(4); episode(22); LT();
#pragma unroll 1
    for (int c = 1; c < 12; ++c) { waitv<18>(); bar(); issCA(c + 4); issCB(c + 4); episode(22 + c); LT(); }
#pragma unroll 1
    for (int d = 0; d < 12; ++d) { waitv<18>(); bar(); issD(d); LT(); }
    waitv<18>(); bar(); LT();
    waitv<9>(); bar(); LT();
    waitv<0>(); bar(); LT();
    bar(); LT();
    asm volatile("" : "+v"(dummy));
    STAMP(10);
  } else {
    unsigned dummy = 0;
    htouch(dummy, wid);
    xload();
    asm volatile("s_waitcnt vmcnt(0)" : "+v"(xr[0]), "+v"(xr[1]), "+v"(xr[2]), "+v"(xr[3]), "+v"(xr[4]), "+v"(xr[5]), "+v"(xr[6]), "+v"(xr[7]), "+v"(xr[8]), "+v"(xr[9]), "+v"(dummy)::"memory");
    STAMP(1);
    layernorm();
    const int cw = wid, wm = cw >> 1, wn = cw & 1;
    const int i = lane & 15, q = lane >> 4, key = (i >> 1) & 7;
    AdA aa;
    {
      const int r0 = wm * 32 + 8 * (i >> 2) + (i & 3);
      aa.a0 = L_XN + r0 * 128 + ((q ^ key) << 4); aa.a1 = aa.a0 ^ 64;
      aa.a0h = aa.a0 + 65536; aa.a1h = aa.a1 + 65536;
      aa.b0 = L_R1 + (wn * 80 + i) * 128 + ((q ^ key) << 4); aa.b1 = aa.b0 ^ 64;
      aa.b0s = aa.b0 + 3 * 20480; aa.b1s = aa.b1 + 3 * 20480;
      asm volatile("" : "+v"(aa.a0), "+v"(aa.a1), "+v"(aa.a0h), "+v"(aa.a1h), "+v"(aa.b0), "+v"(aa.b1), "+v"(aa.b0s), "+v"(aa.b1s));
    }
    bar();
    STAMP(2);
    {
      f32x4 acc[2][5];
      FrA x, y;
      rdA_first(x, aa, mkseq<7>{});
      waitl<0>();
      stageA(acc, x, y, aa, mkseq<10>{});
      STAMP(3);
      mfma_settle();
      const unsigned sg = cb1 < 4 ? 0u : 0x80008000u;
      const unsigned ao = WS_AB + (((cb1 >> 2) * 64 + rb1 * 2 + wm) * 40 + (cb1 & 3) * 10 + wn * 5) * 1024 + lane * 16;
#pragma unroll
      for (int n = 0; n < 5; ++n) {
        u32x4 o = {pk2(acc[0][n][0], acc[0][n][1]) ^ sg, pk2(acc[0][n][2], acc[0][n][3]) ^ sg,
                   pk2(acc[1][n][0], acc[1][n][1]) ^ sg, pk2(acc[1][n][2], acc[1][n][3]) ^ sg};
        gst16<0>(p.ws, ao + n * 1024, o);
      }
    }
    {
      AdB ab;
      const int gq = lane >> 4, qq = (lane >> 2) & 3, pp = lane & 3;
      const int g = ((qq >> 1) & 1) | ((gq & 1) << 1);
#pragma unroll
      for (int n = 0; n < 4; ++n) ab.w[n] = L_R1 + (8 * gq + qq) * 384 + (((6 * wn + n) ^ g) << 5) + pp * 8;
      asm volatile("" : "+v"(ab.w[0]), "+v"(ab.w[1]), "+v"(ab.w[2]), "+v"(ab.w[3]));
      f32x4 acc[2][6];
      FrB x, y;
      rdB_first(x, aa, ab, mkseq<14>{});
      waitl<0>();
      unsigned fv = 0;
      const unsigned fao = WS_FA + (((j >> 1) * 4 + cb2) * 32 + (j & 1) * 16 + (lane & 15)) * 4;
      auto tileB = [&](auto UU) {
        constexpr int U = UU.value;
        halfB<U, 1, true, U == 0>(acc, x, y, aa, ab, mkseq<12>{});
        waitl<0>();
        if constexpr (U == 4) waitv<0>();
        if constexpr (U == 8) { if (cw == 0) flag_wait(fv, p.ws, fao, tok); }
        bar();
        if constexpr (U == 4) { if (cw == 0 && lane == 0) gst4(p.ws, WS_FA + (cb1 * 32 + rb1) * 4, tok); }
        if constexpr (U == 6) { if (cw == 0) flag_req(fv, p.ws, fao); }
        halfB<U + 1, 0, (U < 9), false>(acc, y, x, aa, ab, mkseq<12>{});
        if constexpr (U < 9) waitl<0>();
      };
      [&]<int... U>(iseq<U...>) { (tileB(std::integral_constant<int, U>{}), ...); }(mkseq<10>{});
      STAMP(4);
      mfma_settle();
      const unsigned ho = WS_H + ((rb1 * 48 + cb1 * 6 + wn * 3) * 4 + wm * 2) * 1024 + lane * 16;
#pragma unroll
      for (int m = 0; m < 2; ++m)
#pragma unroll
        for (int pr = 0; pr < 3; ++pr) {
          u32x4 o = {pk2(silu(acc[m][2 * pr][0]), silu(acc[m][2 * pr][1])), pk2(silu(acc[m][2 * pr][2]), silu(acc[m][2 * pr][3])),
                     pk2(silu(acc[m][2 * pr + 1][0]), silu(acc[m][2 * pr + 1][1])), pk2(silu(acc[m][2 * pr + 1][2]), silu(acc[m][2 * pr + 1][3]))};
          gst16<0>(p.ws, ho + (pr * 4 + m) * 1024, o);
        }
      if (blk < 240) {
        bf16x4 t[2][2];
        const unsigned ta = L_W2S + (4 * gq + qq) * 256 + cw * 64 + pp * 8;
        dstr<0>(t[0][0], ta); dstr<4096>(t[0][1], ta); dstr<32>(t[1][0], ta); dstr<4096 + 32>(t[1][1], ta);
        asm volatile("s_waitcnt lgkmcnt(0)" : "+v"(t[0][0]), "+v"(t[0][1]), "+v"(t[1][0]), "+v"(t[1][1]));
#pragma unroll
        for (int b = 0; b < 2; ++b) {
          const int nf = (blk % 5) * 8 + cw * 2 + b;
          const unsigned wo = WS_W2 + (((nf / 5) * 48 + blk / 5) * 5 + nf % 5) * 1024 + lane * 16;
          gst16<0>(p.ws, wo, __builtin_bit_cast(u32x4, cat(t[b][0], t[b][1])));
        }
      }
      STAMP(5);
    }
    {
      AdC ad;
      const int r0 = wm * 64 + 8 * (i >> 2) + (i & 3);
      ad.a0 = L_AR + r0 * 128 + ((q ^ key) << 4); ad.a1 = ad.a0 ^ 64;
      ad.b0 = L_BR + wn * 5120 + lane * 16; ad.b1 = ad.b0 + 3 * 20480;
      asm volatile("" : "+v"(ad.a0), "+v"(ad.a1), "+v"(ad.b0), "+v"(ad.b1));
      f32x4 acc[4][5];
#pragma unroll
      for (int m = 0; m < 4; ++m)
#pragma unroll
        for (int n = 0; n < 5; ++n) { acc[m][n] = f32x4{0, 0, 0, 0}; asm volatile("" : "+v"(acc[m][n])); }
      FrC x, y;
      rdC_first(x, ad, mkseq<9>{});
      waitl<0>();
      u32x4 fv = {0, 0, 0, 0};
      const unsigned fbo = WS_FB + lane * 16;
#pragma unroll 1
      for (int g = 0; g < 4; ++g) {
        halfC0<0>(acc, x, y, ad, mkseq<20>{}); waitl<0>();
        if (g == 3 && cw == 0) flag_wait4(fv, p.ws, fbo, tok);
        if (g == 1) waitv<0>();
        bar();
        if (g == 1 && cw == 0 && lane == 0) gst4(p.ws, WS_FB + blk * 4, tok);
        halfC1<0>(acc, x, y, ad, mkseq<20>{});
        halfC0<1>(acc, x, y, ad, mkseq<20>{}); waitl<0>(); bar();
        halfC1<1>(acc, x, y, ad, mkseq<20>{});
        halfC0<2>(acc, x, y, ad, mkseq<20>{}); waitl<0>(); bar();
        if (g == 2 && cw == 0) flag_req4(fv, p.ws, fbo);
        halfC1<2>(acc, x, y, ad, mkseq<20>{});
        halfC0<3>(acc, x, y, ad, mkseq<20>{}); waitl<0>(); bar();
        halfC1<3>(acc, x, y, ad, mkseq<20>{});
      }
      STAMP(6);
      waitl<0>();
      mfma_settle();
      const unsigned yo = WS_YP + (((rb2 * 4 + cb2) * 4 + cw) * 4 + j) * 10240 + lane * 16;
#pragma unroll
      for (int pr = 0; pr < 2; ++pr)
#pragma unroll
        for (int n = 0; n < 5; ++n) {
          u32x4 o = {pk2(acc[2 * pr][n][0], acc[2 * pr][n][1]), pk2(acc[2 * pr][n][2], acc[2 * pr][n][3]),
                     pk2(acc[2 * pr + 1][n][0], acc[2 * pr + 1][n][1]), pk2(acc[2 * pr + 1][n][2], acc[2 * pr + 1][n][3])};
          gst16<0>(p.ws, yo + (pr * 5 + n) * 1024, o);
        }
      STAMP(7);
    }
    if (wn == 0) stage3<3>(p, cw, lane, blk, wid, tok, rb2, cb2, j);
    else stage3<2>(p, cw, lane, blk, wid, tok, rb2, cb2, j);
    STAMP(10);
  }
}

extern "C" void kernel_launch(void* const* d_in, const int* in_sizes, int n_in, void* d_out, int out_size, void* d_ws, size_t ws_size,
                              hipStream_t stream) {
  Params p;
  p.x = (const char*)d_in[0]; p.cr = (const char*)d_in[1]; p.ci = (const char*)d_in[2];
  p.hr = (const char*)d_in[3]; p.hi = (const char*)d_in[4]; p.w1 = (const char*)d_in[6]; p.w2 = (const char*)d_in[7];
  p.out = (char*)d_out; p.ws = (char*)d_ws;
  hipMemsetAsync((char*)d_ws + WS_FA, 0, 3072, stream);
  fnet_fused<<<256, 512, 0, stream>>>(p);
}
```

```cpp
#include <hip/hip_runtime.h>
#include <stdint.h>
#include <utility>

#define DI __device__ __forceinline__
typedef short bf16x8 __attribute__((ext_vector_type(8)));
typedef short bf16x4 __attribute__((ext_vector_type(4)));
typedef float f32x4 __attribute__((ext_vector_type(4)));
typedef float f32x2 __attribute__((ext_vector_type(2)));
typedef unsigned u32x4 __attribute__((ext_vector_type(4)));
typedef unsigned u32x2 __attribute__((ext_vector_type(2)));
typedef unsigned long long u64;
template <int... I> using iseq = std::integer_sequence<int, I...>;
template <int N> using mkseq = std::make_integer_sequence<int, N>;
template <int N> struct IC { static constexpr int value = N; };
template <class F, int... I> DI void sfor_impl(F&& f, iseq<I...>) { (f(IC<I>{}), ...); }
template <int N, class F> DI void sfor(F&& f) { sfor_impl(f, mkseq<N>{}); }

struct Params {
  const char* x; const char* cr; const char* ci; const char* hr; const char* hi;
  const char* w1; const char* w2; char* out; char* ws;
};

constexpr unsigned WS_AB = 0;
constexpr unsigned WS_H  = WS_AB + 5242880;
constexpr unsigned WS_W2 = WS_H + 6291456;
constexpr unsigned WS_YP = WS_W2 + 1966080;
constexpr unsigned WS_DUM = WS_YP + 10485760;
constexpr unsigned WS_FA = WS_DUM + 8192;
constexpr unsigned WS_FB = WS_FA + 1024;
constexpr unsigned WS_FC = WS_FB + 1024;
constexpr unsigned WS_END = WS_FC + 1024;

constexpr unsigned L_XN = 0;
constexpr unsigned L_R = 81920;
constexpr unsigned L_AR = 0;
constexpr unsigned L_OWN = 65536;
constexpr unsigned L_SP0 = L_R + 36864, L_SP1 = L_R + 77824;
constexpr unsigned L_SIZE = 163840;
constexpr unsigned w1slot(int u, int ks) {
  constexpr unsigned H0 = L_R + 40960, H1 = L_R + 53248, H2 = L_R + 65536, H3 = L_R, H4 = L_R + 12288, H5 = L_R + 24576, E0 = 0, E1 = 12288;
  switch (u) {
    case 0: case 3: case 7: return ks ? H1 : H0;
    case 1: case 4: case 8: return ks ? H3 : H2;
    case 2: case 6: return ks ? H5 : H4;
    default: return ks ? E1 : E0;
  }
}

#define EP0 1
#define EPN 6
#define ROT2 2
#define ROT3 1
#define SPIN_MAX 2000000

template <int OFF> DI void dsr(bf16x8& d, unsigned a) { static_assert(OFF >= 0 && OFF < 65536, ""); asm volatile("ds_read_b128 %0, %1 offset:%2" : "=v"(d) : "v"(a), "i"(OFF)); }
template <int OFF> DI void dsr4(u32x4& d, unsigned a) { static_assert(OFF >= 0 && OFF < 65536, ""); asm volatile("ds_read_b128 %0, %1 offset:%2" : "=v"(d) : "v"(a), "i"(OFF)); }
template <int OFF> DI void dstr(bf16x4& d, unsigned a) { static_assert(OFF >= 0 && OFF < 65536, ""); asm volatile("ds_read_b64_tr_b16 %0, %1 offset:%2" : "=v"(d) : "v"(a), "i"(OFF)); }
template <int OFF> DI void dsw(unsigned a, const u32x4& d) { static_assert(OFF >= 0 && OFF < 65536, ""); asm volatile("ds_write_b128 %0, %1 offset:%2" ::"v"(a), "v"(d), "i"(OFF) : "memory"); }
template <bool Z> DI void mfma(f32x4& c, const bf16x8& a, const bf16x8& b) {
  if constexpr (Z) asm volatile("v_mfma_f32_16x16x32_bf16 %0, %1, %2, 0" : "=&v"(c) : "v"(a), "v"(b));
  else asm volatile("v_mfma_f32_16x16x32_bf16 %0, %1, %2, %0" : "+v"(c) : "v"(a), "v"(b));
}
template <int N> DI void waitl() { static_assert(N >= 0 && N < 16, ""); __builtin_amdgcn_s_waitcnt(0xC07F | (N << 8)); }
template <int N> DI void waitv() { static_assert(N >= 0 && N < 64, ""); asm volatile("s_waitcnt vmcnt(%0)" ::"i"(N) : "memory"); }
DI void bar() { asm volatile("s_barrier" ::: "memory"); }
DI void mfma_settle() { asm volatile("s_nop 7\n\ts_nop 7" ::: "memory"); }
DI void sched_fence() { __builtin_amdgcn_sched_barrier(0); }

#define DMA_HEAD "s_mov_b32 m0, %0\n\ts_nop 0\n\t"
#define DMA4(POL)                                                                                         \
  asm volatile(DMA_HEAD "global_load_lds_dwordx4 %1, %5 " POL "\n\t"                                      \
               "global_load_lds_dwordx4 %2, %5 offset:1024 " POL "\n\t"                                   \
               "global_load_lds_dwordx4 %3, %5 offset:2048 " POL "\n\t"                                   \
               "global_load_lds_dwordx4 %4, %5 offset:3072 " POL ::"s"(m), "v"(v0), "v"(v1), "v"(v2), "v"(v3), "s"(s) : "memory")
template <bool SC1> DI void dma4(unsigned m, unsigned v0, unsigned v1, unsigned v2, unsigned v3, const char* s) {
  if constexpr (SC1) DMA4("sc1"); else DMA4("");
}
#define DMA3(POL)                                                                                         \
  asm volatile(DMA_HEAD "global_load_lds_dwordx4 %1, %4 " POL "\n\t"                                      \
               "global_load_lds_dwordx4 %2, %4 offset:1024 " POL "\n\t"                                   \
               "global_load_lds_dwordx4 %3, %4 offset:2048 " POL ::"s"(m), "v"(v0), "v"(v1), "v"(v2), "s"(s) : "memory")
template <bool SC1> DI void dma3(unsigned m, unsigned v0, unsigned v1, unsigned v2, const char* s) {
  if constexpr (SC1) DMA3("sc1"); else DMA3("");
}
#define DMA2(POL)                                                                                         \
  asm volatile(DMA_HEAD "global_load_lds_dwordx4 %1, %3 " POL "\n\t"                                      \
               "global_load_lds_dwordx4 %2, %3 offset:1024 " POL ::"s"(m), "v"(v0), "v"(v1), "s"(s) : "memory")
template <bool SC1> DI void dma2(unsigned m, unsigned v0, unsigned v1, const char* s) {
  if constexpr (SC1) DMA2("sc1"); else DMA2("");
}
#define DMA1(POL) asm volatile(DMA_HEAD "global_load_lds_dwordx4 %1, %2 " POL ::"s"(m), "v"(v0), "s"(s) : "memory")
template <bool SC1> DI void dma1(unsigned m, unsigned v0, const char* s) {
  if constexpr (SC1) DMA1("sc1"); else DMA1("");
}

template <int OFF> DI void gst16(const char* base, unsigned voff, const u32x4& d) {
  static_assert(OFF >= 0 && OFF < 4096, "");
  asm volatile("global_store_dwordx4 %0, %1, %2 offset:%3 sc1\n\ts_nop 1" ::"v"(voff), "v"(d), "s"(base), "i"(OFF) : "memory");
}
DI void gst4(const char* base, unsigned voff, unsigned d) {
  asm volatile("global_store_dword %0, %1, %2 sc1" ::"v"(voff), "v"(d), "s"(base) : "memory");
}
DI unsigned pk2(float lo, float hi) { unsigned r; asm("v_cvt_pk_bf16_f32 %0, %1, %2" : "=v"(r) : "v"(lo), "v"(hi)); return r; }
DI float bflo(unsigned v) { return __uint_as_float(v << 16); }
DI float bfhi(unsigned v) { return __uint_as_float(v & 0xffff0000u); }
DI f32x2 bfpair(unsigned v) { return f32x2{bflo(v), bfhi(v)}; }
template <int CTRL> DI float dppadd(float v) {
  return v + __int_as_float(__builtin_amdgcn_update_dpp(0, __float_as_int(v), CTRL, 0xf, 0xf, true));
}
DI float red8(float v) { v = dppadd<0xB1>(v); v = dppadd<0x4E>(v); return dppadd<0x141>(v); }
template <int N> DI void silu_pairs(f32x2 (&v)[N]) {
  f32x2 t[N];
#pragma unroll
  for (int k = 0; k < N; ++k) t[k] = v[k] * f32x2{-1.44269504088896341f, -1.44269504088896341f};
  sched_fence();
#pragma unroll
  for (int k = 0; k < N; ++k) { t[k][0] = __builtin_amdgcn_exp2f(t[k][0]); t[k][1] = __builtin_amdgcn_exp2f(t[k][1]); }
  sched_fence();
#pragma unroll
  for (int k = 0; k < N; ++k) t[k] += f32x2{1.0f, 1.0f};
  sched_fence();
#pragma unroll
  for (int k = 0; k < N; ++k) { t[k][0] = __builtin_amdgcn_rcpf(t[k][0]); t[k][1] = __builtin_amdgcn_rcpf(t[k][1]); }
  sched_fence();
#pragma unroll
  for (int k = 0; k < N; ++k) v[k] *= t[k];
  sched_fence();
}

DI void touch(unsigned& d, const char* a) { asm volatile("global_load_dword %0, %1, off" : "+v"(d) : "v"(a) : "memory"); }
DI void touchs(unsigned& d, unsigned voff, const char* base) { asm volatile("global_load_dword %0, %1, %2" : "+v"(d) : "v"(voff), "s"(base) : "memory"); }
DI void touch2_if(unsigned& d, int n, int ep, unsigned voff, const char* b0, const char* b1) {
  asm volatile("s_cmp_lg_u32 %1, %2\n\ts_cbranch_scc1 .Lskip_touch%=\n\t"
               "global_load_dword %0, %3, %4\n\tglobal_load_dword %0, %3, %5\n"
               ".Lskip_touch%=:" : "+v"(d) : "s"(n), "s"(ep), "v"(voff), "s"(b0), "s"(b1) : "memory", "scc");
}

DI void poll_req(unsigned& fv, const char* base, unsigned voff) {
  asm volatile("global_load_dword %0, %1, %2 sc1" : "+v"(fv) : "v"(voff), "s"(base) : "memory");
}
DI void poll_req4(u32x4& fv, const char* base, unsigned voff) {
  asm volatile("global_load_dwordx4 %0, %1, %2 sc1" : "+v"(fv) : "v"(voff), "s"(base) : "memory");
}
DI void poll_spin(unsigned& fv, const char* base, unsigned voff, unsigned tok) {
  asm volatile("" : "+v"(fv));
  for (int n = 0; __builtin_amdgcn_ballot_w64(fv != tok) != 0 && n < SPIN_MAX; ++n)
    asm volatile("global_load_dword %0, %1, %2 sc1\n\ts_waitcnt vmcnt(0)" : "=v"(fv) : "v"(voff), "s"(base) : "memory");
}
DI void poll_spin4(u32x4& fv, const char* base, unsigned voff, unsigned tok) {
  asm volatile("" : "+v"(fv));
  for (int n = 0; __builtin_amdgcn_ballot_w64(fv[0] != tok || fv[1] != tok || fv[2] != tok || fv[3] != tok) != 0 && n < SPIN_MAX; ++n)
    asm volatile("global_load_dwordx4 %0, %1, %2 sc1\n\ts_waitcnt vmcnt(0)" : "=v"(fv) : "v"(voff), "s"(base) : "memory");
}

#define LT() do {} while (0)
#define RT() do {} while (0)
#define DIAG_DECL do {} while (0)
#define DIAG_OUT() do {} while (0)
#define DIAG_ARGS
#define DIAG_PASS

struct FrB { bf16x8 a[2]; bf16x4 w[6][2]; };
struct AdX { unsigned a0, a1, a0h, a1h; };
struct AdB { unsigned e[4], r[4], r2[4]; };
template <int I, int T, int KS> DI void rdX(bf16x8& d, const AdX& ad) {
  dsr<(T & 7) * 8192 + I * 512>(d, T < 8 ? (KS ? ad.a1 : ad.a0) : (KS ? ad.a1h : ad.a0h));
}
template <int I, int U, int KS> DI void rdB(FrB& f, const AdX& ax, const AdB& ad) {
  if constexpr (I < 2) rdX<I, U, KS>(f.a[I], ax);
  else {
    constexpr int n = (I - 2) >> 1, h = (I - 2) & 1;
    constexpr unsigned base = w1slot(U, KS), in = h * 1536 + (n >> 2) * 128;
    if constexpr (base < L_R) dstr<base + in>(f.w[n][h], ad.e[n & 3]);
    else if constexpr (base - L_R < 65536) dstr<base - L_R + in>(f.w[n][h], ad.r[n & 3]);
    else dstr<base - L_R - 65536 + in>(f.w[n][h], ad.r2[n & 3]);
  }
}

struct FrA { bf16x8 a[2], b[5]; };
struct AdA { unsigned p0, p1, p0s, p1s, s0, s1; };
template <int I, int T, int KS> DI void rdA(FrA& f, const AdX& ax, const AdA& ad) {
  if constexpr (I < 2) rdX<I, T, KS>(f.a[I], ax);
  else {
    constexpr int n = I - 2, s = T & 3;
    if constexpr (n == 4) dsr<s * 20480>(f.b[4], KS ? ad.s1 : ad.s0);
    else {
      constexpr int in = (n >> 1) * 4096 + (n & 1) * 512;
      if constexpr (s < 3) dsr<s * 20480 + in>(f.b[n], KS ? ad.p1 : ad.p0);
      else dsr<in>(f.b[n], KS ? ad.p1s : ad.p0s);
    }
  }
}
template <int G, int T, int KS> DI void gapA(FrA& y, const AdX& ax, const AdA& ad) {
  if constexpr (G < 4) { rdA<2 * G, T, KS>(y, ax, ad); if constexpr (2 * G + 1 < 7) rdA<2 * G + 1, T, KS>(y, ax, ad); }
}
template <int T, int KS, bool Z, int... G> DI void halfA(f32x4 (&acc)[2][5], FrA& x, FrA& y, const AdX& ax, const AdA& ad, iseq<G...>) {
  ((mfma<Z>(acc[G / 5][G % 5], x.a[G / 5], x.b[G % 5]), gapA<G, T, KS>(y, ax, ad)), ...);
}
template <int G> DI void gapAB(FrB& xb, const AdX& ax, const AdB& ab) {
  if constexpr (G < 7) { rdB<2 * G, 0, 0>(xb, ax, ab); rdB<2 * G + 1, 0, 0>(xb, ax, ab); }
}
template <int... G> DI void halfA_last(f32x4 (&acc)[2][5], FrA& y, FrB& xb, const AdX& ax, const AdB& ab, iseq<G...>) {
  ((mfma<false>(acc[G / 5][G % 5], y.a[G / 5], y.b[G % 5]), gapAB<G>(xb, ax, ab)), ...);
}
template <int... I> DI void rdA_first(FrA& x, const AdX& ax, const AdA& ad, iseq<I...>) { (rdA<I, 0, 0>(x, ax, ad), ...); }

template <int G, int U, int KS, bool RD> DI void gapB(FrB& y, const AdX& ax, const AdB& ad) {
  if constexpr (RD && G < 7) { rdB<2 * G, U, KS>(y, ax, ad); rdB<2 * G + 1, U, KS>(y, ax, ad); }
}
DI bf16x8 cat(const bf16x4& l, const bf16x4& h) { return __builtin_shufflevector(l, h, 0, 1, 2, 3, 4, 5, 6, 7); }
template <int U, int KS, bool RD, bool Z, int... G> DI void halfB(f32x4 (&acc)[2][6], FrB& x, FrB& y, const AdX& ax, const AdB& ad, iseq<G...>) {
  ((mfma<Z>(acc[G / 6][G % 6], cat(x.w[G % 6][0], x.w[G % 6][1]), x.a[G / 6]), gapB<G, U, KS, RD>(y, ax, ad)), ...);
}

struct FrC { bf16x8 a[4], b[5]; };
struct AdC { unsigned a0, a1, b0, b1; };
template <int I, int S, int KS> DI void rdC(FrC& f, const AdC& ad) {
  if constexpr (I >= 1 && I <= 5) {
    constexpr int n = I - 1, off = S * 20480 + KS * 10240 + n * 1024;
    if constexpr (S < 3) dsr<off>(f.b[n], ad.b0); else dsr<off - 3 * 20480>(f.b[n], ad.b1);
  } else {
    constexpr int m = (I == 0) ? 0 : I - 5, off = ((S + 2) & 3) * 16384 + (m >> 1) * 4096 + (m & 1) * 512;
    dsr<off>(f.a[m], KS ? ad.a1 : ad.a0);
  }
}
template <int G, int S> DI void gapC0(FrC& y, const AdC& ad) {
  if constexpr (G < 9) rdC<G, S, 1>(y, ad);
  if constexpr (G == 4) waitl<7>();
  if constexpr (G == 9) waitl<10>();
  if constexpr (G == 14) waitl<9>();
}
template <int G, int S> DI void gapC1(FrC& x, const AdC& ad) {
  if constexpr (G < 9) rdC<G, (S + 1) & 3, 0>(x, ad);
  if constexpr (G == 19) waitl<3>();
}
template <int S, int... G> DI void halfC0(f32x4 (&acc)[4][5], FrC& x, FrC& y, const AdC& ad, iseq<G...>) {
  ((mfma<false>(acc[G / 5][G % 5], x.b[G % 5], x.a[G / 5]), gapC0<G, S>(y, ad)), ...);
}
template <int S, int... G> DI void halfC1(f32x4 (&acc)[4][5], FrC& x, FrC& y, const AdC& ad, iseq<G...>) {
  ((mfma<false>(acc[G / 5][G % 5], y.b[G % 5], y.a[G / 5]), gapC1<G, S>(x, ad)), ...);
}
template <int... I> DI void rdC_first(FrC& x, const AdC& ad, iseq<I...>) { (rdC<I, 0, 0>(x, ad), ...); }

template <int NN> struct FrD { bf16x8 h[2][2], w[2][NN]; };
struct AdD { unsigned a, b, bs; };
template <int NN> constexpr int nfrag(int k) { return NN == 3 ? (k == 2 ? 4 : k) : 2 + k; }
template <int NN, int I, int D, int HF> DI void rdD(FrD<NN>& f, const AdD& ad) {
  constexpr int kk = I / (NN + 2), r = I % (NN + 2), ks = HF * 2 + kk;
  if constexpr (r < 2) dsr<((D + 2) & 3) * 16384 + ks * 4096 + r * 1024>(f.h[kk][r], ad.a);
  else if constexpr ((D & 3) < 3) dsr<(D & 3) * 20480 + ks * 5120 + nfrag<NN>(r - 2) * 1024>(f.w[kk][r - 2], ad.b);
  else dsr<ks * 5120 + nfrag<NN>(r - 2) * 1024>(f.w[kk][r - 2], ad.bs);
}
template <int NN, int G, int D, int HF> DI void gapD(FrD<NN>& y, const AdD& ad) {
  if constexpr (2 * G < 2 * (NN + 2)) rdD<NN, 2 * G, D, HF>(y, ad);
  if constexpr (2 * G + 1 < 2 * (NN + 2)) rdD<NN, 2 * G + 1, D, HF>(y, ad);
}
template <int NN, int G> DI void mfD(f32x4 (&acc)[2][NN], FrD<NN>& x) {
  constexpr int kk = G / (2 * NN), m = (G % (2 * NN)) / NN, n = G % NN;
  mfma<false>(acc[m][n], x.w[kk][n], x.h[kk][m]);
}
template <int NN, int D, int HF, int... G> DI void halfD(f32x4 (&acc)[2][NN], FrD<NN>& x, FrD<NN>& y, const AdD& ad, iseq<G...>) {
  ((mfD<NN, G>(acc, x), gapD<NN, G, D, HF>(y, ad)), ...);
}
template <int NN, int G> DI void gapCD(FrD<NN>& x, const AdD& ad) { if constexpr (G < 2 * (NN + 2)) rdD<NN, G, 0, 0>(x, ad); }
template <int NN, int... G> DI void halfC_last(f32x4 (&acc)[4][5], FrC& y, FrD<NN>& xd, const AdD& ad, iseq<G...>) {
  ((mfma<false>(acc[G / 5][G % 5], y.b[G % 5], y.a[G / 5]), gapCD<NN, G>(xd, ad)), ...);
}
template <int NN, int G> DI void gapY(u32x4 (&yp)[4][NN], const unsigned (&ya)[4]) {
  if constexpr (G < 2 * NN) {
    constexpr int i0 = 2 * G, i1 = 2 * G + 1;
    dsr4<nfrag<NN>(i0 % NN) * 1024>(yp[i0 / NN][i0 % NN], ya[i0 / NN]);
    dsr4<nfrag<NN>(i1 % NN) * 1024>(yp[i1 / NN][i1 % NN], ya[i1 / NN]);
  }
}
template <int NN, int... G> DI void halfD_last(f32x4 (&acc)[2][NN], FrD<NN>& y, u32x4 (&yp)[4][NN], const unsigned (&ya)[4], iseq<G...>) {
  ((mfD<NN, G>(acc, y), gapY<NN, G>(yp, ya)), ...);
}

template <int NN>
DI void stage23(const Params& p, int cw, int lane, int blk, int wid, unsigned tok, int rb2, int cb2, int j DIAG_ARGS) {
  const int wm3 = cw >> 1;
  const int tile = rb2 * 4 + cb2;
  const int i = lane & 15, q = lane >> 4, par = j & 1;
  f32x4 acc2[4][5];
  FrC xc, yc;
  {
    AdC ad;
    const int wm = cw >> 1, wn = NN == 3 ? 0 : 1;
    const int key2 = ((i >> 1) & 1) | ((i >> 2) << 1);
    const int r0 = wm * 64 + 8 * (i >> 2) + (i & 3);
    ad.a0 = L_AR + r0 * 128 + ((q ^ key2) << 4); ad.a1 = ad.a0 ^ 64;
    ad.b0 = L_R + wn * 5120 + lane * 16; ad.b1 = ad.b0 + 3 * 20480;
    asm volatile("" : "+v"(ad.a0), "+v"(ad.a1), "+v"(ad.b0), "+v"(ad.b1));
#pragma unroll
    for (int m = 0; m < 4; ++m)
#pragma unroll
      for (int n = 0; n < 5; ++n) { acc2[m][n] = f32x4{0, 0, 0, 0}; asm volatile("" : "+v"(acc2[m][n])); }
    bar();
    LT();
    rdC_first(xc, ad, mkseq<9>{});
    waitl<0>();
#pragma unroll 1
    for (int g = 0; g < 4; ++g) {
      halfC0<0>(acc2, xc, yc, ad, mkseq<20>{}); waitl<0>();
      if (g == 1) waitv<0>();
      bar();
      if (g == 1 && cw == 0 && lane == 0) gst4(p.ws, WS_FB + blk * 4, tok);
      halfC1<0>(acc2, xc, yc, ad, mkseq<20>{});
      halfC0<1>(acc2, xc, yc, ad, mkseq<20>{}); waitl<0>(); bar();
      halfC1<1>(acc2, xc, yc, ad, mkseq<20>{});
      halfC0<2>(acc2, xc, yc, ad, mkseq<20>{}); waitl<0>(); bar();
      halfC1<2>(acc2, xc, yc, ad, mkseq<20>{});
      halfC0<3>(acc2, xc, yc, ad, mkseq<20>{}); waitl<0>(); bar();
      if (g < 3) halfC1<3>(acc2, xc, yc, ad, mkseq<20>{});
    }
  }
  AdD ad;
  ad.a = L_AR + wm3 * 2048 + lane * 16;
  ad.b = L_R + lane * 16;
  ad.bs = ad.b + 3 * 20480;
  asm volatile("" : "+v"(ad.a), "+v"(ad.b), "+v"(ad.bs));
  FrD<NN> x, y;
  halfC_last<NN>(acc2, yc, x, ad, mkseq<20>{});
  LT();
  mfma_settle();
  if (cw == j) {
    const unsigned a = L_OWN + lane * 16;
    sfor<10>([&](auto K) {
      constexpr int pr = K.value / 5, n = K.value % 5;
      u32x4 o = {pk2(acc2[2 * pr][n][0], acc2[2 * pr][n][1]), pk2(acc2[2 * pr][n][2], acc2[2 * pr][n][3]),
                 pk2(acc2[2 * pr + 1][n][0], acc2[2 * pr + 1][n][1]), pk2(acc2[2 * pr + 1][n][2], acc2[2 * pr + 1][n][3])};
      dsw<K.value * 1024>(a, o);
    });
  } else {
    unsigned yo = WS_YP + ((tile * 4 + cw) * 4 + j) * 10240 + lane * 16;
    sfor<10>([&](auto K) {
      constexpr int pr = K.value / 5, n = K.value % 5;
      u32x4 o = {pk2(acc2[2 * pr][n][0], acc2[2 * pr][n][1]), pk2(acc2[2 * pr][n][2], acc2[2 * pr][n][3]),
                 pk2(acc2[2 * pr + 1][n][0], acc2[2 * pr + 1][n][1]), pk2(acc2[2 * pr + 1][n][2], acc2[2 * pr + 1][n][3])};
      if constexpr (K.value == 4 || K.value == 8) { yo += 4096; asm volatile("" : "+v"(yo)); }
      gst16<(K.value & 3) * 1024>(p.ws, yo, o);
    });
  }
  f32x4 acc[2][NN];
#pragma unroll
  for (int m = 0; m < 2; ++m)
#pragma unroll
    for (int n = 0; n < NN; ++n) { acc[m][n] = f32x4{0, 0, 0, 0}; asm volatile("" : "+v"(acc[m][n])); }
  waitl<0>();
  LT();
  const unsigned trow = rb2 * 128 + 64 * (j >> 1) + 32 * wm3 + 8 * (i >> 2) + (i & 3);
  const unsigned ecol = cb2 * 160 + 80 * par;
  const unsigned xop = trow * 1280 + (ecol + (NN == 3 ? (par ? 16 : 0) : (par ? 48 : 32))) * 2 + q * 16;
  const unsigned xos = trow * 1280 + (ecol + (par ? 0 : 64)) * 2 + q * 8;
  unsigned ya[4];
#pragma unroll
  for (int jj = 0; jj < 4; ++jj) {
    const int k = (jj - j - 1) & 3;
    ya[jj] = (k == 0 ? L_R : k == 1 ? L_R + 10240 : k == 2 ? 32768u : L_OWN) + wm3 * 5120 + lane * 16;
    asm volatile("" : "+v"(ya[jj]));
  }
  u32x4 xp[2];
  u32x2 xs[2];
  u32x4 yp[4][NN];
  sfor<12>([&](auto DD) {
    constexpr int D = DD.value;
    halfD<NN, D, 1>(acc, x, y, ad, mkseq<4 * NN>{});
    waitl<0>();
    if constexpr (D == 3) waitv<0>();
    bar();
    if constexpr (D == 3) { if (cw == 0 && lane == 0) gst4(p.ws, WS_FC + (tile * 4 + j) * 4, tok); }
    if constexpr (D == 8) {
      asm volatile("global_load_dwordx4 %0, %2, %3\n\tglobal_load_dwordx4 %1, %4, %3" : "=&v"(xp[0]), "=&v"(xp[1]) : "v"(xop), "s"(p.x), "v"(xop + 4 * 1280) : "memory");
      if constexpr (NN == 3)
        asm volatile("global_load_dwordx2 %0, %2, %3\n\tglobal_load_dwordx2 %1, %4, %3" : "=&v"(xs[0]), "=&v"(xs[1]) : "v"(xos), "s"(p.x), "v"(xos + 4 * 1280) : "memory");
    }
    if constexpr (D < 11) { halfD<NN, D + 1, 0>(acc, y, x, ad, mkseq<4 * NN>{}); waitl<0>(); }
    else halfD_last<NN>(acc, y, yp, ya, mkseq<4 * NN>{});
  });
  waitl<0>();
  LT();
  if constexpr (NN == 3) asm volatile("s_waitcnt vmcnt(0)" : "+v"(xp[0]), "+v"(xp[1]), "+v"(xs[0]), "+v"(xs[1])::"memory");
  else asm volatile("s_waitcnt vmcnt(0)" : "+v"(xp[0]), "+v"(xp[1])::"memory");
#pragma unroll
  for (int jj = 0; jj < 4; ++jj)
#pragma unroll
    for (int n = 0; n < NN; ++n) asm volatile("" : "+v"(yp[jj][n]));
  f32x2 ys[2][NN][2];
#pragma unroll
  for (int m = 0; m < 2; ++m)
#pragma unroll
    for (int n = 0; n < NN; ++n)
#pragma unroll
      for (int h = 0; h < 2; ++h)
        ys[m][n][h] = (bfpair(yp[0][n][m * 2 + h]) + bfpair(yp[1][n][m * 2 + h])) - (bfpair(yp[2][n][m * 2 + h]) + bfpair(yp[3][n][m * 2 + h]));
  sched_fence();
  mfma_settle();
  f32x2 o[2 * NN * 2];
#pragma unroll
  for (int m = 0; m < 2; ++m)
#pragma unroll
    for (int n = 0; n < NN; ++n) { o[(m * NN + n) * 2] = f32x2{acc[m][n][0], acc[m][n][1]}; o[(m * NN + n) * 2 + 1] = f32x2{acc[m][n][2], acc[m][n][3]}; }
  silu_pairs<2 * NN * 2>(o);
#pragma unroll
  for (int m = 0; m < 2; ++m) {
    u32x4 r;
#pragma unroll
    for (int n = 0; n < 2; ++n)
#pragma unroll
      for (int h = 0; h < 2; ++h) {
        f32x2 t = bfpair(xp[m][n * 2 + h]) + ys[m][n][h] * o[(m * NN + n) * 2 + h];
        r[n * 2 + h] = pk2(t[0], t[1]);
      }
    asm volatile("global_store_dwordx4 %0, %1, %2 sc1\n\ts_nop 1" ::"v"(xop + m * 4 * 1280), "v"(r), "s"(p.out) : "memory");
    if constexpr (NN == 3) {
      u32x2 r2;
#pragma unroll
      for (int h = 0; h < 2; ++h) {
        f32x2 t = bfpair(xs[m][h]) + ys[m][2][h] * o[(m * NN + 2) * 2 + h];
        r2[h] = pk2(t[0], t[1]);
      }
      asm volatile("global_store_dwordx2 %0, %1, %2 sc1\n\ts_nop 1" ::"v"(xos + m * 4 * 1280), "v"(r2), "s"(p.out) : "memory");
    }
  }
  LT();
}

__global__ __launch_bounds__(512, 2) void fnet_fused(Params p) {
  __shared__ __attribute__((aligned(16))) char lds[L_SIZE];
  const int tid = threadIdx.x, lane = tid & 63;
  const int wid = __builtin_amdgcn_readfirstlane(tid >> 6);
  const int blk = blockIdx.x, xcd = blk & 7, idx = blk >> 3;
  const int rb1 = (xcd & 1) * 16 + (idx >> 1), cb1 = (xcd >> 1) * 2 + (idx & 1);
  const int j = xcd & 3, rb2 = (xcd >> 2) * 8 + (idx >> 2), cb2 = idx & 3;
  const unsigned tok = 1;
  asm volatile("" ::"v"((unsigned)(size_t)lds));
  DIAG_DECL;
  RT(); LT();

  if (wid >= 4) {
    const int lw = wid - 4;
    unsigned dummy = 0;
    {
      const char* hb = ((xcd >> 1) < 2 ? p.hr : p.hi) + (size_t)((xcd >> 1) & 1) * 320 * 1280;
      touchs(dummy, (idx * 100 + lw * 25 + (lane < 25 ? lane : 24)) * 128, hb);
    }
    unsigned va[5];
#pragma unroll
    for (int u = 0; u < 5; ++u) {
      const int row = lw * 40 + u * 8 + (lane >> 3);
      const int key = (row >> 5) == 2 ? ((row >> 1) & 7) : (((row >> 1) & 1) | (((row >> 3) & 3) << 1));
      va[u] = row * 1280 + (((lane & 7) ^ key) << 4) - (u < 4 ? u * 1024 : 0);
    }
    const char* ha = (cb1 < 4 ? p.hr : p.hi) + (size_t)(cb1 & 3) * 160 * 1280;
    auto issA = [&](int t) {
      const unsigned m = L_R + (t & 3) * 20480 + lw * 5120;
      dma4<false>(m, va[0], va[1], va[2], va[3], ha + t * 128);
      dma1<false>(m + 4096, va[4], ha + t * 128);
    };
    unsigned vb[3];
#pragma unroll
    for (int u = 0; u < 3; ++u) {
      const int G = u * 64 + lane, row = lw * 8 + G / 24, c = G % 24;
      const int g = ((row >> 1) & 1) | (((row >> 3) & 1) << 1);
      vb[u] = row * 3072 + ((c ^ (g << 1)) << 4) - u * 1024;
    }
    const char* wb = p.w1 + cb1 * 384;
    auto issW = [&](int u, int ks) { dma3<false>(w1slot(u, ks) + lw * 3072, vb[0], vb[1], vb[2], wb + (size_t)(u * 64 + ks * 32) * 3072); };
    const int ep = EP0 + (((idx >> 1) * EPN) >> 2);
    const char* tb = (j < 2 ? p.cr : p.ci) + (size_t)((xcd >> 2) * 1024 + (idx & 1) * 512 + lw * 128) * 4096 + (j & 1) * 2048 + (((idx >> 1) + ROT2 * ((idx & 1) * 4 + lw)) & 15) * 128;
    const unsigned tvo = lane * 4096;
    auto step = [&](int n) {
      touch2_if(dummy, n, ep, tvo, tb, tb + 64 * 4096);
      LT();
    };
    const int pb = blk < 240 ? blk : blk - 16;
    const char* w2p = p.w2 + __builtin_amdgcn_readfirstlane((pb / 5) * 32 * 1280 + (pb % 5) * 256);
    asm volatile("s_waitcnt vmcnt(0)" : "+v"(dummy)::"memory");
    LT();
    issA(0); issA(1); issA(2); issA(3);
    {
      const int n = idx * 120 + lw * 30 + (lane < 30 ? lane : 29);
      const int l2 = lane < 48 ? lane : 47;
      const int r = lw * 8 + ((l2 - 32) >> 1);
      const char* a = lane < 32 ? p.w1 + (size_t)(n / 6) * 3072 + (xcd >> 1) * 768 + (n % 6) * 128
                                : w2p + r * 1280 + (l2 & 1) * 128;
      touch(dummy, a);
    }
    waitv<16>();
    bar(); step(1);
    waitv<10>(); bar(); issA(4); step(2);
    waitv<10>(); bar(); issA(5); step(3);
    waitv<10>(); bar(); issA(6); step(4);
    waitv<10>(); bar(); issA(7); step(5);
    waitv<10>(); bar(); issA(8); step(6);
    waitv<10>(); bar(); issA(9); step(7);
    waitv<10>(); bar(); issW(0, 0); step(8);
    waitv<8>();  bar(); issW(0, 1); issW(1, 0); step(9);
    waitv<9>();  bar(); issW(1, 1); step(10);
    waitv<6>();  bar(); issW(2, 0); issW(2, 1); step(11);
    unsigned vc[4];
#pragma unroll
    for (int u = 0; u < 4; ++u) {
      const int row = 8 * u + (lane >> 3), key = ((row >> 1) & 1) | (u << 1);
      vc[u] = row * 4096 + (((lane & 7) ^ key) << 4) - u * 1024;
    }
    const char* ca = (j < 2 ? p.cr : p.ci) + (size_t)(rb2 * 128 + lw * 32) * 4096 + (j & 1) * 2048;
    const unsigned vl = lane * 16;
    const char* cb = p.ws + WS_AB + (size_t)(j * 32 * 40 + cb2 * 10) * 1024 + (lw >> 1) * 40960 + (lw & 1) * 5120;
    const int rot2 = ROT2 * (idx >> 2);
    auto issCA = [&](int t) { dma4<false>(L_AR + ((t + 2) & 3) * 16384 + lw * 4096, vc[0], vc[1], vc[2], vc[3], ca + ((t + rot2) & 15) * 128); };
    auto issCB = [&](int t) {
      const unsigned m = L_R + (t & 3) * 20480 + lw * 5120;
      const char* s = cb + (size_t)((t + rot2) & 15) * 81920;
      dma4<true>(m, vl, vl, vl, vl, s);
      dma1<true>(m + 4096, vl, s + 4096);
    };
    const unsigned fao = WS_FA + (((j >> 1) * 4 + cb2) * 32 + (j & 1) * 16 + (lane & 15)) * 4;
    unsigned fa1 = 0, fa2 = 0;
    waitv<6>();  bar(); issW(3, 0); issW(3, 1); step(12);
    waitv<6>();  bar(); issW(4, 0); issW(4, 1); issW(5, 0);
    {
      const unsigned vw = (lw * 8 + (lane >> 4)) * 1280 + (lane & 15) * 16;
      dma2<false>((lw < 2 ? L_SP0 : L_SP1 - 4096) + lw * 2048, vw, vw + 4 * 1280 - 1024, w2p);
    }
    step(13);
    waitv<11>(); bar(); issW(5, 1); issW(6, 0); issW(6, 1); step(14);
    waitv<14>(); bar(); issW(7, 0); issW(7, 1); step(15);
    waitv<12>(); bar(); issW(8, 0); issW(8, 1); step(16);
    waitv<12>(); bar(); issW(9, 0); issW(9, 1); step(17);
    waitv<12>(); bar(); issCA(0); step(18);
    waitv<10>(); bar(); issCA(1); poll_req(fa1, p.ws, fao); step(19);
    waitv<9>();  bar(); poll_req(fa2, p.ws, fao); step(20);
    bar();
    asm volatile("s_waitcnt vmcnt(1)" : "+v"(fa1)::"memory");
    if (__builtin_amdgcn_ballot_w64(fa1 != tok) != 0) {
      asm volatile("s_waitcnt vmcnt(0)" : "+v"(fa2)::"memory");
      poll_spin(fa2, p.ws, fao, tok);
    }
    issCB(0); issCB(1); issCA(2); issCB(2); issCA(3); issCB(3);
    step(21);
    waitv<23>(); bar(); step(22);
    const char* da = p.ws + WS_H + (size_t)(rb2 * 2 + (j >> 1)) * 48 * 4096 + lw * 4096;
    const char* db = p.ws + WS_W2 + (size_t)(cb2 * 2 + (j & 1)) * 48 * 5120 + lw * 5120;
    const int rot3 = ROT3 * (3 * cb2 + ((3 * (idx >> 2)) >> 1));
    auto issD = [&](int d) {
      const int dr = (d + rot3) % 12;
      dma4<true>(L_AR + ((d + 2) & 3) * 16384 + lw * 4096, vl, vl, vl, vl, da + (size_t)dr * 16384);
      const unsigned m = L_R + (d & 3) * 20480 + lw * 5120;
      const char* s = db + (size_t)dr * 20480;
      dma4<true>(m, vl, vl, vl, vl, s);
      dma1<true>(m + 4096, vl, s + 4096);
    };
    u32x4 fb = {0, 0, 0, 0};
    const unsigned fbo = WS_FB + lane * 16;
    sfor<12>([&](auto CC) {
      constexpr int c = CC.value;
      waitv<18>(); bar(); issCA(c + 4); issCB(c + 4);
      if constexpr (c == 9) poll_req4(fb, p.ws, fbo);
      step(23 + c);
    });
    waitv<18>(); bar();
    poll_spin4(fb, p.ws, fbo, tok);
    issD(0); LT();
    waitv<18>(); bar(); issD(1); LT();
    waitv<18>(); bar(); issD(2); LT();
    waitv<18>(); bar(); issD(3); LT();
    const int tile = rb2 * 4 + cb2;
    unsigned fc = 0;
    const unsigned fco = WS_FC + (tile * 4 + (lane & 3)) * 4;
    sfor<8>([&](auto DD) {
      constexpr int d = DD.value;
      waitv<18>(); bar(); issD(d + 4);
      if constexpr (d == 6) poll_req(fc, p.ws, fco);
      LT();
    });
    waitv<18>(); bar();
    asm volatile("s_waitcnt vmcnt(9)" : "+v"(fc)::"memory");
    poll_spin(fc, p.ws, fco, tok);
    if (lw < 3) {
      const char* s = p.ws + WS_YP + (size_t)((tile * 4 + j) * 4 + ((j + 1 + lw) & 3)) * 10240;
      const unsigned m = lw == 0 ? L_R : lw == 1 ? L_R + 10240 : 32768u;
      dma4<true>(m, vl, vl, vl, vl, s);
      dma4<true>(m + 4096, vl, vl, vl, vl, s + 4096);
      dma2<true>(m + 8192, vl, vl, s + 8192);
    }
    LT();
    bar(); LT();
    if (lw < 3) waitv<10>(); else waitv<0>();
    bar(); LT();
    waitv<0>(); bar(); LT();
    asm volatile("" : "+v"(dummy));
    DIAG_OUT();
  } else {
    const int cw = wid, wm = cw >> 1, wn = cw & 1;
    __builtin_amdgcn_s_setprio(3);
    {
      u32x4 xr[2][10];
      const unsigned xo = (rb1 * 64 + cw * 16 + (lane >> 3)) * 1280 + (lane & 7) * 16;
#define XLOAD(B, O)                                                                                                                   \
      asm volatile(                                                                                                                   \
          "global_load_dwordx4 %0, %10, %11\n\tglobal_load_dwordx4 %1, %10, %11 offset:128\n\t"                                       \
          "global_load_dwordx4 %2, %10, %11 offset:256\n\tglobal_load_dwordx4 %3, %10, %11 offset:384\n\t"                            \
          "global_load_dwordx4 %4, %10, %11 offset:512\n\tglobal_load_dwordx4 %5, %10, %11 offset:640\n\t"                            \
          "global_load_dwordx4 %6, %10, %11 offset:768\n\tglobal_load_dwordx4 %7, %10, %11 offset:896\n\t"                            \
          "global_load_dwordx4 %8, %10, %11 offset:1024\n\tglobal_load_dwordx4 %9, %10, %11 offset:1152"                              \
          : "=&v"(xr[B][0]), "=&v"(xr[B][1]), "=&v"(xr[B][2]), "=&v"(xr[B][3]), "=&v"(xr[B][4]), "=&v"(xr[B][5]), "=&v"(xr[B][6]),    \
            "=&v"(xr[B][7]), "=&v"(xr[B][8]), "=&v"(xr[B][9])                                                                         \
          : "v"(O), "s"(p.x) : "memory")
      XLOAD(0, xo);
      XLOAD(1, xo + 8 * 1280);
      sfor<2>([&](auto BB) {
        constexpr int B = BB.value;
        f32x2 v[40];
        f32x2 s = {0.f, 0.f};
        sfor<10>([&](auto KK) {
          constexpr int k = KK.value;
          asm volatile("s_waitcnt vmcnt(%2)" : "+v"(xr[B][k]), "+v"(s) : "i"(19 - B * 10 - k) : "memory");
#pragma unroll
          for (int e = 0; e < 4; ++e) { v[k * 4 + e] = bfpair(xr[B][k][e]); s += v[k * 4 + e]; }
        });
        if constexpr (B == 0) LT();
        const float mean = red8(s[0] + s[1]) * (1.0f / 640.0f);
        f32x2 s2a = {0.f, 0.f}, s2b = {0.f, 0.f};
#pragma unroll
        for (int k = 0; k < 40; k += 2) {
          v[k] -= f32x2{mean, mean}; s2a += v[k] * v[k];
          v[k + 1] -= f32x2{mean, mean}; s2b += v[k + 1] * v[k + 1];
        }
        s2a += s2b;
        const float rstd = __builtin_amdgcn_rsqf(red8(s2a[0] + s2a[1]) * (1.0f / 640.0f) + 1e-5f);
        const int row = cw * 16 + B * 8 + (lane >> 3);
        const int key = ((row >> 1) & 1) | (((row >> 3) & 3) << 1);
        const unsigned a = L_XN + row * 128 + (((lane & 7) ^ key) << 4);
        const unsigned ah = a + 65536;
        sfor<10>([&](auto KK) {
          constexpr int k = KK.value;
          u32x4 o;
#pragma unroll
          for (int e = 0; e < 4; ++e) { f32x2 t = v[k * 4 + e] * f32x2{rstd, rstd}; o[e] = pk2(t[0], t[1]); }
          if constexpr (k < 8) dsw<k * 8192>(a, o); else dsw<(k - 8) * 8192>(ah, o);
        });
      });
    }
    const int i = lane & 15, q = lane >> 4;
    AdX ax; AdA aa;
    {
      const int key2 = ((i >> 1) & 1) | ((i >> 2) << 1);
      const int r0 = wm * 32 + 8 * (i >> 2) + (i & 3);
      ax.a0 = L_XN + r0 * 128 + ((q ^ key2) << 4); ax.a1 = ax.a0 ^ 64;
      ax.a0h = ax.a0 + 65536; ax.a1h = ax.a1 + 65536;
      aa.p0 = L_R + ((wn ? 96 : 0) + 8 * (i >> 2) + (i & 3)) * 128 + ((q ^ key2) << 4); aa.p1 = aa.p0 ^ 64;
      aa.p0s = aa.p0 + 3 * 20480; aa.p1s = aa.p1 + 3 * 20480;
      aa.s0 = L_R + ((wn ? 80 : 64) + i) * 128 + ((q ^ (i >> 1)) << 4); aa.s1 = aa.s0 ^ 64;
      asm volatile("" : "+v"(ax.a0), "+v"(ax.a1), "+v"(ax.a0h), "+v"(ax.a1h));
      asm volatile("" : "+v"(aa.p0), "+v"(aa.p1), "+v"(aa.p0s), "+v"(aa.p1s), "+v"(aa.s0), "+v"(aa.s1));
    }
    const int gq = lane >> 4, qq = (lane >> 2) & 3, pp = lane & 3;
    AdB ab;
    {
      const int g = ((qq >> 1) & 1) | ((gq & 1) << 1);
#pragma unroll
      for (int n = 0; n < 4; ++n) {
        ab.e[n] = (8 * gq + qq) * 384 + (((6 * wn + n) ^ g) << 5) + pp * 8;
        ab.r[n] = ab.e[n] + L_R; ab.r2[n] = ab.r[n] + 65536;
        asm volatile("" : "+v"(ab.e[n]), "+v"(ab.r[n]), "+v"(ab.r2[n]));
      }
    }
    waitl<0>();
    LT();
    bar();
    LT();
    FrB xb, yb;
    {
      f32x4 acc[2][5];
      FrA x, y;
      rdA_first(x, ax, aa, mkseq<7>{});
      waitl<0>();
      sfor<10>([&](auto TT) {
        constexpr int T = TT.value;
        halfA<T, 1, T == 0>(acc, x, y, ax, aa, mkseq<10>{});
        waitl<0>(); bar();
        if constexpr (T < 9) halfA<T + 1, 0, false>(acc, y, x, ax, aa, mkseq<10>{});
        else halfA_last(acc, y, xb, ax, ab, mkseq<10>{});
        waitl<0>();
      });
      LT();
      mfma_settle();
      unsigned ao = WS_AB + (((cb1 >> 2) * 64 + rb1 * 2 + wm) * 40 + (cb1 & 3) * 10 + wn * 5) * 1024 + lane * 16;
      sfor<5>([&](auto NN_) {
        constexpr int n = NN_.value;
        u32x4 o = {pk2(acc[0][n][0], acc[0][n][1]), pk2(acc[0][n][2], acc[0][n][3]), pk2(acc[1][n][0], acc[1][n][1]), pk2(acc[1][n][2], acc[1][n][3])};
        if constexpr (n == 4) { ao += 4096; asm volatile("" : "+v"(ao)); }
        gst16<(n & 3) * 1024>(p.ws, ao, o);
      });
      LT();
    }
    {
      f32x4 acc[2][6];
      sfor<10>([&](auto UU) {
        constexpr int U = UU.value;
        halfB<U, 1, true, U == 0>(acc, xb, yb, ax, ab, mkseq<12>{});
        waitl<0>();
        if constexpr (U == 4) waitv<0>();
        bar();
        if constexpr (U == 4) {
          if (cw == 0 && lane == 0) gst4(p.ws, WS_FA + (cb1 * 32 + rb1) * 4, tok);
          const int B5 = (blk % 5) * 4 + cw, r5 = B5 % 5, dg = B5 / 5;
          const bool single = r5 == 2;
          const unsigned ta = L_SP0 + (4 * gq + qq) * 256 + cw * 64 + (single ? pp * 8 : pp * 16);
          const unsigned tb2 = ta + (single ? 32 : 8);
          bf16x4 t[2][2];
          dstr<0>(t[0][0], ta); dstr<L_SP1 - L_SP0>(t[0][1], ta); dstr<0>(t[1][0], tb2); dstr<L_SP1 - L_SP0>(t[1][1], tb2);
          asm volatile("s_waitcnt lgkmcnt(0)" : "+v"(t[0][0]), "+v"(t[0][1]), "+v"(t[1][0]), "+v"(t[1][1]));
#pragma unroll
          for (int b = 0; b < 2; ++b) {
            const int qg = 2 * dg + (r5 > 2 ? 1 : (single ? b : 0));
            const int n = single ? 4 : ((r5 == 1 || r5 == 4) ? 2 + b : b);
            const unsigned wo = (blk < 240 ? WS_W2 + ((qg * 48 + blk / 5) * 5 + n) * 1024 : WS_DUM + (cw * 2 + b) * 1024) + lane * 16;
            gst16<0>(p.ws, wo, __builtin_bit_cast(u32x4, cat(t[b][0], t[b][1])));
          }
        }
        halfB<U + 1, 0, (U < 9), false>(acc, yb, xb, ax, ab, mkseq<12>{});
        if constexpr (U < 9) waitl<0>();
      });
      LT();
      mfma_settle();
      const unsigned ho = WS_H + ((rb1 * 48 + cb1 * 6 + wn * 3) * 4 + wm * 2) * 1024 + lane * 16;
      sfor<2>([&](auto MM) {
        constexpr int m = MM.value;
        f32x2 v[12];
#pragma unroll
        for (int n = 0; n < 6; ++n) { v[2 * n] = f32x2{acc[m][n][0], acc[m][n][1]}; v[2 * n + 1] = f32x2{acc[m][n][2], acc[m][n][3]}; }
        silu_pairs<12>(v);
#pragma unroll
        for (int pr = 0; pr < 3; ++pr) {
          u32x4 o = {pk2(v[4 * pr][0], v[4 * pr][1]), pk2(v[4 * pr + 1][0], v[4 * pr + 1][1]), pk2(v[4 * pr + 2][0], v[4 * pr + 2][1]), pk2(v[4 * pr + 3][0], v[4 * pr + 3][1])};
          gst16<m * 1024>(p.ws, ho + pr * 4096, o);
        }
      });
      LT();
    }
    if (wn == 0) stage23<3>(p, cw, lane, blk, wid, tok, rb2, cb2, j DIAG_PASS);
    else stage23<2>(p, cw, lane, blk, wid, tok, rb2, cb2, j DIAG_PASS);
    DIAG_OUT();
  }
}

extern "C" void kernel_launch(void* const* d_in, const int* in_sizes, int n_in, void* d_out, int out_size, void* d_ws, size_t ws_size,
                              hipStream_t stream) {
  Params p;
  p.x = (const char*)d_in[0]; p.cr = (const char*)d_in[1]; p.ci = (const char*)d_in[2];
  p.hr = (const char*)d_in[3]; p.hi = (const char*)d_in[4]; p.w1 = (const char*)d_in[6]; p.w2 = (const char*)d_in[7];
  p.out = (char*)d_out; p.ws = (char*)d_ws;

  fnet_fused<<<256, 512, 0, stream>>>(p);
}
```

```cpp
#define SPINT 5000
#include <hip/hip_runtime.h>
#include <stdint.h>
#include <utility>

#define DI __device__ __forceinline__
typedef short bf16x8 __attribute__((ext_vector_type(8)));
typedef short bf16x4 __attribute__((ext_vector_type(4)));
typedef float f32x4 __attribute__((ext_vector_type(4)));
typedef float f32x2 __attribute__((ext_vector_type(2)));
typedef unsigned u32x4 __attribute__((ext_vector_type(4)));
typedef unsigned u32x2 __attribute__((ext_vector_type(2)));
typedef unsigned long long u64;
template <int... I> using iseq = std::integer_sequence<int, I...>;
template <int N> using mkseq = std::make_integer_sequence<int, N>;
template <int N> struct IC { static constexpr int value = N; };
template <class F, int... I> DI void sfor_impl(F&& f, iseq<I...>) { (f(IC<I>{}), ...); }
template <int N, class F> DI void sfor(F&& f) { sfor_impl(f, mkseq<N>{}); }

struct Params {
  const char* x; const char* cr; const char* ci; const char* hr; const char* hi;
  const char* w1; const char* w2; char* out; char* ws;
};

constexpr unsigned WS_AB = 0;
constexpr unsigned WS_H  = WS_AB + 5242880;
constexpr unsigned WS_W2 = WS_H + 6291456;
constexpr unsigned WS_YP = WS_W2 + 1966080;
constexpr unsigned WS_DUM = WS_YP + 10485760;
constexpr unsigned WS_FA = WS_DUM + 8192;
constexpr unsigned WS_FB = WS_FA + 1024;
constexpr unsigned WS_FC = WS_FB + 1024;
constexpr unsigned WS_END = WS_FC + 1024;

constexpr unsigned L_XN = 0;
constexpr unsigned L_R = 81920;
constexpr unsigned L_AR = 0;
constexpr unsigned L_OWN = 65536;
constexpr unsigned L_SP0 = L_R + 36864, L_SP1 = L_R + 77824;
constexpr unsigned L_SIZE = 163840;
constexpr unsigned w1slot(int u, int ks) {
  constexpr unsigned H0 = L_R + 40960, H1 = L_R + 53248, H2 = L_R + 65536, H3 = L_R, H4 = L_R + 12288, H5 = L_R + 24576, E0 = 0, E1 = 12288;
  switch (u) {
    case 0: case 3: case 7: return ks ? H1 : H0;
    case 1: case 4: case 8: return ks ? H3 : H2;
    case 2: case 6: return ks ? H5 : H4;
    default: return ks ? E1 : E0;
  }
}

#define EP0 1
#define EPN 6
#define ROT2 2
#define ROT3 1
#define SPIN_MAX 2000000

template <int OFF> DI void dsr(bf16x8& d, unsigned a) { static_assert(OFF >= 0 && OFF < 65536, ""); asm volatile("ds_read_b128 %0, %1 offset:%2" : "=v"(d) : "v"(a), "i"(OFF)); }
template <int OFF> DI void dsr4(u32x4& d, unsigned a) { static_assert(OFF >= 0 && OFF < 65536, ""); asm volatile("ds_read_b128 %0, %1 offset:%2" : "=v"(d) : "v"(a), "i"(OFF)); }
template <int OFF> DI void dstr(bf16x4& d, unsigned a) { static_assert(OFF >= 0 && OFF < 65536, ""); asm volatile("ds_read_b64_tr_b16 %0, %1 offset:%2" : "=v"(d) : "v"(a), "i"(OFF)); }
template <int OFF> DI void dsw(unsigned a, const u32x4& d) { static_assert(OFF >= 0 && OFF < 65536, ""); asm volatile("ds_write_b128 %0, %1 offset:%2" ::"v"(a), "v"(d), "i"(OFF) : "memory"); }
template <bool Z> DI void mfma(f32x4& c, const bf16x8& a, const bf16x8& b) {
  if constexpr (Z) asm volatile("v_mfma_f32_16x16x32_bf16 %0, %1, %2, 0" : "=&v"(c) : "v"(a), "v"(b));
  else asm volatile("v_mfma_f32_16x16x32_bf16 %0, %1, %2, %0" : "+v"(c) : "v"(a), "v"(b));
}
template <int N> DI void waitl() { static_assert(N >= 0 && N < 16, ""); __builtin_amdgcn_s_waitcnt(0xC07F | (N << 8)); }
template <int N> DI void waitv() { static_assert(N >= 0 && N < 64, ""); asm volatile("s_waitcnt vmcnt(%0)" ::"i"(N) : "memory"); }
DI void bar() { asm volatile("s_barrier" ::: "memory"); }
DI void mfma_settle() { asm volatile("s_nop 7\n\ts_nop 7" ::: "memory"); }
DI void sched_fence() { __builtin_amdgcn_sched_barrier(0); }

#define DMA_HEAD "s_mov_b32 m0, %0\n\ts_nop 0\n\t"
#define DMA4(POL)                                                                                         \
  asm volatile(DMA_HEAD "global_load_lds_dwordx4 %1, %5 " POL "\n\t"                                      \
               "global_load_lds_dwordx4 %2, %5 offset:1024 " POL "\n\t"                                   \
               "global_load_lds_dwordx4 %3, %5 offset:2048 " POL "\n\t"                                   \
               "global_load_lds_dwordx4 %4, %5 offset:3072 " POL ::"s"(m), "v"(v0), "v"(v1), "v"(v2), "v"(v3), "s"(s) : "memory")
template <bool SC1> DI void dma4(unsigned m, unsigned v0, unsigned v1, unsigned v2, unsigned v3, const char* s) {
  if constexpr (SC1) DMA4("sc1"); else DMA4("");
}
#define DMA3(POL)                                                                                         \
  asm volatile(DMA_HEAD "global_load_lds_dwordx4 %1, %4 " POL "\n\t"                                      \
               "global_load_lds_dwordx4 %2, %4 offset:1024 " POL "\n\t"                                   \
               "global_load_lds_dwordx4 %3, %4 offset:2048 " POL ::"s"(m), "v"(v0), "v"(v1), "v"(v2), "s"(s) : "memory")
template <bool SC1> DI void dma3(unsigned m, unsigned v0, unsigned v1, unsigned v2, const char* s) {
  if constexpr (SC1) DMA3("sc1"); else DMA3("");
}
#define DMA2(POL)                                                                                         \
  asm volatile(DMA_HEAD "global_load_lds_dwordx4 %1, %3 " POL "\n\t"                                      \
               "global_load_lds_dwordx4 %2, %3 offset:1024 " POL ::"s"(m), "v"(v0), "v"(v1), "s"(s) : "memory")
template <bool SC1> DI void dma2(unsigned m, unsigned v0, unsigned v1, const char* s) {
  if constexpr (SC1) DMA2("sc1"); else DMA2("");
}
#define DMA1(POL) asm volatile(DMA_HEAD "global_load_lds_dwordx4 %1, %2 " POL ::"s"(m), "v"(v0), "s"(s) : "memory")
template <bool SC1> DI void dma1(unsigned m, unsigned v0, const char* s) {
  if constexpr (SC1) DMA1("sc1"); else DMA1("");
}

template <int OFF> DI void gst16(const char* base, unsigned voff, const u32x4& d) {
  static_assert(OFF >= 0 && OFF < 4096, "");
  asm volatile("global_store_dwordx4 %0, %1, %2 offset:%3 sc1\n\ts_nop 1" ::"v"(voff), "v"(d), "s"(base), "i"(OFF) : "memory");
}
DI void gst4(const char* base, unsigned voff, unsigned d) {
  asm volatile("global_store_dword %0, %1, %2 sc1" ::"v"(voff), "v"(d), "s"(base) : "memory");
}
DI unsigned pk2(float lo, float hi) { unsigned r; asm("v_cvt_pk_bf16_f32 %0, %1, %2" : "=v"(r) : "v"(lo), "v"(hi)); return r; }
DI float bflo(unsigned v) { return __uint_as_float(v << 16); }
DI float bfhi(unsigned v) { return __uint_as_float(v & 0xffff0000u); }
DI f32x2 bfpair(unsigned v) { return f32x2{bflo(v), bfhi(v)}; }
template <int CTRL> DI float dppadd(float v) {
  return v + __int_as_float(__builtin_amdgcn_update_dpp(0, __float_as_int(v), CTRL, 0xf, 0xf, true));
}
DI float red8(float v) { v = dppadd<0xB1>(v); v = dppadd<0x4E>(v); return dppadd<0x141>(v); }
template <int N> DI void silu_pairs(f32x2 (&v)[N]) {
  f32x2 t[N];
#pragma unroll
  for (int k = 0; k < N; ++k) t[k] = v[k] * f32x2{-1.44269504088896341f, -1.44269504088896341f};
  sched_fence();
#pragma unroll
  for (int k = 0; k < N; ++k) { t[k][0] = __builtin_amdgcn_exp2f(t[k][0]); t[k][1] = __builtin_amdgcn_exp2f(t[k][1]); }
  sched_fence();
#pragma unroll
  for (int k = 0; k < N; ++k) t[k] += f32x2{1.0f, 1.0f};
  sched_fence();
#pragma unroll
  for (int k = 0; k < N; ++k) { t[k][0] = __builtin_amdgcn_rcpf(t[k][0]); t[k][1] = __builtin_amdgcn_rcpf(t[k][1]); }
  sched_fence();
#pragma unroll
  for (int k = 0; k < N; ++k) v[k] *= t[k];
  sched_fence();
}

DI void touch(unsigned& d, const char* a) { asm volatile("global_load_dword %0, %1, off" : "+v"(d) : "v"(a) : "memory"); }
DI void touchs(unsigned& d, unsigned voff, const char* base) { asm volatile("global_load_dword %0, %1, %2" : "+v"(d) : "v"(voff), "s"(base) : "memory"); }
DI void touch2_if(unsigned& d, int n, int ep, unsigned voff, const char* b0, const char* b1) {
  asm volatile("s_cmp_lg_u32 %1, %2\n\ts_cbranch_scc1 .Lskip_touch%=\n\t"
               "global_load_dword %0, %3, %4\n\tglobal_load_dword %0, %3, %5\n"
               ".Lskip_touch%=:" : "+v"(d) : "s"(n), "s"(ep), "v"(voff), "s"(b0), "s"(b1) : "memory", "scc");
}

DI void poll_req(unsigned& fv, const char* base, unsigned voff) {
  asm volatile("global_load_dword %0, %1, %2 sc1" : "+v"(fv) : "v"(voff), "s"(base) : "memory");
}
DI void poll_req4(u32x4& fv, const char* base, unsigned voff) {
  asm volatile("global_load_dwordx4 %0, %1, %2 sc1" : "+v"(fv) : "v"(voff), "s"(base) : "memory");
}
DI void poll_spin(unsigned& fv, const char* base, unsigned voff, unsigned tok) {
  asm volatile("" : "+v"(fv));
  for (int n = 0; __builtin_amdgcn_ballot_w64(fv != tok) != 0 && n < SPIN_MAX; ++n)
    asm volatile("global_load_dword %0, %1, %2 sc1\n\ts_waitcnt vmcnt(0)" : "=v"(fv) : "v"(voff), "s"(base) : "memory");
}
DI void poll_spin4(u32x4& fv, const char* base, unsigned voff, unsigned tok) {
  asm volatile("" : "+v"(fv));
  for (int n = 0; __builtin_amdgcn_ballot_w64(fv[0] != tok || fv[1] != tok || fv[2] != tok || fv[3] != tok) != 0 && n < SPIN_MAX; ++n)
    asm volatile("global_load_dwordx4 %0, %1, %2 sc1\n\ts_waitcnt vmcnt(0)" : "=v"(fv) : "v"(voff), "s"(base) : "memory");
}

#define LT() do {} while (0)
#define RT() do {} while (0)
#define DIAG_DECL do {} while (0)
#define DIAG_OUT() do {} while (0)
#define DIAG_ARGS
#define DIAG_PASS

struct FrB { bf16x8 a[2]; bf16x4 w[6][2]; };
struct AdX { unsigned a0, a1, a0h, a1h; };
struct AdB { unsigned e[4], r[4], r2[4]; };
template <int I, int T, int KS> DI void rdX(bf16x8& d, const AdX& ad) {
  dsr<(T & 7) * 8192 + I * 512>(d, T < 8 ? (KS ? ad.a1 : ad.a0) : (KS ? ad.a1h : ad.a0h));
}
template <int I, int U, int KS> DI void rdB(FrB& f, const AdX& ax, const AdB& ad) {
  if constexpr (I < 2) rdX<I, U, KS>(f.a[I], ax);
  else {
    constexpr int n = (I - 2) >> 1, h = (I - 2) & 1;
    constexpr unsigned base = w1slot(U, KS), in = h * 1536 + (n >> 2) * 128;
    if constexpr (base < L_R) dstr<base + in>(f.w[n][h], ad.e[n & 3]);
    else if constexpr (base - L_R < 65536) dstr<base - L_R + in>(f.w[n][h], ad.r[n & 3]);
    else dstr<base - L_R - 65536 + in>(f.w[n][h], ad.r2[n & 3]);
  }
}

struct FrA { bf16x8 a[2], b[5]; };
struct AdA { unsigned p0, p1, p0s, p1s, s0, s1; };
template <int I, int T, int KS> DI void rdA(FrA& f, const AdX& ax, const AdA& ad) {
  if constexpr (I < 2) rdX<I, T, KS>(f.a[I], ax);
  else {
    constexpr int n = I - 2, s = T & 3;
    if constexpr (n == 4) dsr<s * 20480>(f.b[4], KS ? ad.s1 : ad.s0);
    else {
      constexpr int in = (n >> 1) * 4096 + (n & 1) * 512;
      if constexpr (s < 3) dsr<s * 20480 + in>(f.b[n], KS ? ad.p1 : ad.p0);
      else dsr<in>(f.b[n], KS ? ad.p1s : ad.p0s);
    }
  }
}
template <int G, int T, int KS> DI void gapA(FrA& y, const AdX& ax, const AdA& ad) {
  if constexpr (G < 4) { rdA<2 * G, T, KS>(y, ax, ad); if constexpr (2 * G + 1 < 7) rdA<2 * G + 1, T, KS>(y, ax, ad); }
}
template <int T, int KS, bool Z, int... G> DI void halfA(f32x4 (&acc)[2][5], FrA& x, FrA& y, const AdX& ax, const AdA& ad, iseq<G...>) {
  ((mfma<Z>(acc[G / 5][G % 5], x.a[G / 5], x.b[G % 5]), gapA<G, T, KS>(y, ax, ad)), ...);
}
template <int G> DI void gapAB(FrB& xb, const AdX& ax, const AdB& ab) {
  if constexpr (G < 7) { rdB<2 * G, 0, 0>(xb, ax, ab); rdB<2 * G + 1, 0, 0>(xb, ax, ab); }
}
template <int... G> DI void halfA_last(f32x4 (&acc)[2][5], FrA& y, FrB& xb, const AdX& ax, const AdB& ab, iseq<G...>) {
  ((mfma<false>(acc[G / 5][G % 5], y.a[G / 5], y.b[G % 5]), gapAB<G>(xb, ax, ab)), ...);
}
template <int... I> DI void rdA_first(FrA& x, const AdX& ax, const AdA& ad, iseq<I...>) { (rdA<I, 0, 0>(x, ax, ad), ...); }

template <int G, int U, int KS, bool RD> DI void gapB(FrB& y, const AdX& ax, const AdB& ad) {
  if constexpr (RD && G < 7) { rdB<2 * G, U, KS>(y, ax, ad); rdB<2 * G + 1, U, KS>(y, ax, ad); }
}
DI bf16x8 cat(const bf16x4& l, const bf16x4& h) { return __builtin_shufflevector(l, h, 0, 1, 2, 3, 4, 5, 6, 7); }
template <int U, int KS, bool RD, bool Z, int... G> DI void halfB(f32x4 (&acc)[2][6], FrB& x, FrB& y, const AdX& ax, const AdB& ad, iseq<G...>) {
  ((mfma<Z>(acc[G / 6][G % 6], cat(x.w[G % 6][0], x.w[G % 6][1]), x.a[G / 6]), gapB<G, U, KS, RD>(y, ax, ad)), ...);
}

struct FrC { bf16x8 a[4], b[5]; };
struct AdC { unsigned a0, a1, b0, b1; };
template <int I, int S, int KS> DI void rdC(FrC& f, const AdC& ad) {
  if constexpr (I >= 1 && I <= 5) {
    constexpr int n = I - 1, off = S * 20480 + KS * 10240 + n * 1024;
    if constexpr (S < 3) dsr<off>(f.b[n], ad.b0); else dsr<off - 3 * 20480>(f.b[n], ad.b1);
  } else {
    constexpr int m = (I == 0) ? 0 : I - 5, off = ((S + 2) & 3) * 16384 + (m >> 1) * 4096 + (m & 1) * 512;
    dsr<off>(f.a[m], KS ? ad.a1 : ad.a0);
  }
}
template <int G, int S> DI void gapC0(FrC& y, const AdC& ad) {
  if constexpr (G < 9) rdC<G, S, 1>(y, ad);
  if constexpr (G == 4) waitl<7>();
  if constexpr (G == 9) waitl<10>();
  if constexpr (G == 14) waitl<9>();
}
template <int G, int S> DI void gapC1(FrC& x, const AdC& ad) {
  if constexpr (G < 9) rdC<G, (S + 1) & 3, 0>(x, ad);
  if constexpr (G == 19) waitl<3>();
}
template <int S, int... G> DI void halfC0(f32x4 (&acc)[4][5], FrC& x, FrC& y, const AdC& ad, iseq<G...>) {
  ((mfma<false>(acc[G / 5][G % 5], x.b[G % 5], x.a[G / 5]), gapC0<G, S>(y, ad)), ...);
}
template <int S, int... G> DI void halfC1(f32x4 (&acc)[4][5], FrC& x, FrC& y, const AdC& ad, iseq<G...>) {
  ((mfma<false>(acc[G / 5][G % 5], y.b[G % 5], y.a[G / 5]), gapC1<G, S>(x, ad)), ...);
}
template <int... I> DI void rdC_first(FrC& x, const AdC& ad, iseq<I...>) { (rdC<I, 0, 0>(x, ad), ...); }

template <int NN> struct FrD { bf16x8 h[2][2], w[2][NN]; };
struct AdD { unsigned a, b, bs; };
template <int NN> constexpr int nfrag(int k) { return NN == 3 ? (k == 2 ? 4 : k) : 2 + k; }
template <int NN, int I, int D, int HF> DI void rdD(FrD<NN>& f, const AdD& ad) {
  constexpr int kk = I / (NN + 2), r = I % (NN + 2), ks = HF * 2 + kk;
  if constexpr (r < 2) dsr<((D + 2) & 3) * 16384 + ks * 4096 + r * 1024>(f.h[kk][r], ad.a);
  else if constexpr ((D & 3) < 3) dsr<(D & 3) * 20480 + ks * 5120 + nfrag<NN>(r - 2) * 1024>(f.w[kk][r - 2], ad.b);
  else dsr<ks * 5120 + nfrag<NN>(r - 2) * 1024>(f.w[kk][r - 2], ad.bs);
}
template <int NN, int G, int D, int HF> DI void gapD(FrD<NN>& y, const AdD& ad) {
  if constexpr (2 * G < 2 * (NN + 2)) rdD<NN, 2 * G, D, HF>(y, ad);
  if constexpr (2 * G + 1 < 2 * (NN + 2)) rdD<NN, 2 * G + 1, D, HF>(y, ad);
}
template <int NN, int G> DI void mfD(f32x4 (&acc)[2][NN], FrD<NN>& x) {
  constexpr int kk = G / (2 * NN), m = (G % (2 * NN)) / NN, n = G % NN;
  mfma<false>(acc[m][n], x.w[kk][n], x.h[kk][m]);
}
template <int NN, int D, int HF, int... G> DI void halfD(f32x4 (&acc)[2][NN], FrD<NN>& x, FrD<NN>& y, const AdD& ad, iseq<G...>) {
  ((mfD<NN, G>(acc, x), gapD<NN, G, D, HF>(y, ad)), ...);
}
template <int NN, int G> DI void gapCD(FrD<NN>& x, const AdD& ad) { if constexpr (G < 2 * (NN + 2)) rdD<NN, G, 0, 0>(x, ad); }
template <int NN, int... G> DI void halfC_last(f32x4 (&acc)[4][5], FrC& y, FrD<NN>& xd, const AdD& ad, iseq<G...>) {
  ((mfma<false>(acc[G / 5][G % 5], y.b[G % 5], y.a[G / 5]), gapCD<NN, G>(xd, ad)), ...);
}
template <int NN, int G> DI void gapY(u32x4 (&yp)[4][NN], const unsigned (&ya)[4]) {
  if constexpr (G < 2 * NN) {
    constexpr int i0 = 2 * G, i1 = 2 * G + 1;
    dsr4<nfrag<NN>(i0 % NN) * 1024>(yp[i0 / NN][i0 % NN], ya[i0 / NN]);
    dsr4<nfrag<NN>(i1 % NN) * 1024>(yp[i1 / NN][i1 % NN], ya[i1 / NN]);
  }
}
template <int NN, int... G> DI void halfD_last(f32x4 (&acc)[2][NN], FrD<NN>& y, u32x4 (&yp)[4][NN], const unsigned (&ya)[4], iseq<G...>) {
  ((mfD<NN, G>(acc, y), gapY<NN, G>(yp, ya)), ...);
}

template <int NN>
DI void stage23(const Params& p, int cw, int lane, int blk, int wid, unsigned tok, int rb2, int cb2, int j DIAG_ARGS) {
  const int wm3 = cw >> 1;
  const int tile = rb2 * 4 + cb2;
  const int i = lane & 15, q = lane >> 4, par = j & 1;
  f32x4 acc2[4][5];
  FrC xc, yc;
  {
    AdC ad;
    const int wm = cw >> 1, wn = NN == 3 ? 0 : 1;
    const int key2 = ((i >> 1) & 1) | ((i >> 2) << 1);
    const int r0 = wm * 64 + 8 * (i >> 2) + (i & 3);
    ad.a0 = L_AR + r0 * 128 + ((q ^ key2) << 4); ad.a1 = ad.a0 ^ 64;
    ad.b0 = L_R + wn * 5120 + lane * 16; ad.b1 = ad.b0 + 3 * 20480;
    asm volatile("" : "+v"(ad.a0), "+v"(ad.a1), "+v"(ad.b0), "+v"(ad.b1));
#pragma unroll
    for (int m = 0; m < 4; ++m)
#pragma unroll
      for (int n = 0; n < 5; ++n) { acc2[m][n] = f32x4{0, 0, 0, 0}; asm volatile("" : "+v"(acc2[m][n])); }
    bar();
    LT();
    rdC_first(xc, ad, mkseq<9>{});
    waitl<0>();
#pragma unroll 1
    for (int g = 0; g < 4; ++g) {
      halfC0<0>(acc2, xc, yc, ad, mkseq<20>{}); waitl<0>();
      if (g == 1) waitv<0>();
      bar();
      if (g == 1 && cw == 0 && lane == 0) gst4(p.ws, WS_FB + blk * 4, tok);
      halfC1<0>(acc2, xc, yc, ad, mkseq<20>{});
      halfC0<1>(acc2, xc, yc, ad, mkseq<20>{}); waitl<0>(); bar();
      halfC1<1>(acc2, xc, yc, ad, mkseq<20>{});
      halfC0<2>(acc2, xc, yc, ad, mkseq<20>{}); waitl<0>(); bar();
      halfC1<2>(acc2, xc, yc, ad, mkseq<20>{});
      halfC0<3>(acc2, xc, yc, ad, mkseq<20>{}); waitl<0>(); bar();
      if (g < 3) halfC1<3>(acc2, xc, yc, ad, mkseq<20>{});
    }
  }
  AdD ad;
  ad.a = L_AR + wm3 * 2048 + lane * 16;
  ad.b = L_R + lane * 16;
  ad.bs = ad.b + 3 * 20480;
  asm volatile("" : "+v"(ad.a), "+v"(ad.b), "+v"(ad.bs));
  FrD<NN> x, y;
  halfC_last<NN>(acc2, yc, x, ad, mkseq<20>{});
  LT();
  mfma_settle();
  if (cw == j) {
    const unsigned a = L_OWN + lane * 16;
    sfor<10>([&](auto K) {
      constexpr int pr = K.value / 5, n = K.value % 5;
      u32x4 o = {pk2(acc2[2 * pr][n][0], acc2[2 * pr][n][1]), pk2(acc2[2 * pr][n][2], acc2[2 * pr][n][3]),
                 pk2(acc2[2 * pr + 1][n][0], acc2[2 * pr + 1][n][1]), pk2(acc2[2 * pr + 1][n][2], acc2[2 * pr + 1][n][3])};
      dsw<K.value * 1024>(a, o);
    });
  } else {
    unsigned yo = WS_YP + ((tile * 4 + cw) * 4 + j) * 10240 + lane * 16;
    sfor<10>([&](auto K) {
      constexpr int pr = K.value / 5, n = K.value % 5;
      u32x4 o = {pk2(acc2[2 * pr][n][0], acc2[2 * pr][n][1]), pk2(acc2[2 * pr][n][2], acc2[2 * pr][n][3]),
                 pk2(acc2[2 * pr + 1][n][0], acc2[2 * pr + 1][n][1]), pk2(acc2[2 * pr + 1][n][2], acc2[2 * pr + 1][n][3])};
      if constexpr (K.value == 4 || K.value == 8) { yo += 4096; asm volatile("" : "+v"(yo)); }
      gst16<(K.value & 3) * 1024>(p.ws, yo, o);
    });
  }
  f32x4 acc[2][NN];
#pragma unroll
  for (int m = 0; m < 2; ++m)
#pragma unroll
    for (int n = 0; n < NN; ++n) { acc[m][n] = f32x4{0, 0, 0, 0}; asm volatile("" : "+v"(acc[m][n])); }
  waitl<0>();
  LT();
  const unsigned trow = rb2 * 128 + 64 * (j >> 1) + 32 * wm3 + 8 * (i >> 2) + (i & 3);
  const unsigned ecol = cb2 * 160 + 80 * par;
  const unsigned xop = trow * 1280 + (ecol + (NN == 3 ? (par ? 16 : 0) : (par ? 48 : 32))) * 2 + q * 16;
  const unsigned xos = trow * 1280 + (ecol + (par ? 0 : 64)) * 2 + q * 8;
  unsigned ya[4];
#pragma unroll
  for (int jj = 0; jj < 4; ++jj) {
    const int k = (jj - j - 1) & 3;
    ya[jj] = (k == 0 ? L_R : k == 1 ? L_R + 10240 : k == 2 ? 32768u : L_OWN) + wm3 * 5120 + lane * 16;
    asm volatile("" : "+v"(ya[jj]));
  }
  u32x4 xp[2];
  u32x2 xs[2];
  u32x4 yp[4][NN];
  sfor<12>([&](auto DD) {
    constexpr int D = DD.value;
    halfD<NN, D, 1>(acc, x, y, ad, mkseq<4 * NN>{});
    waitl<0>();
    if constexpr (D == 3) waitv<0>();
    bar();
    if constexpr (D == 3) { if (cw == 0 && lane == 0) gst4(p.ws, WS_FC + (tile * 4 + j) * 4, tok); }
    if constexpr (D == 8) {
      asm volatile("global_load_dwordx4 %0, %2, %3\n\tglobal_load_dwordx4 %1, %4, %3" : "=&v"(xp[0]), "=&v"(xp[1]) : "v"(xop), "s"(p.x), "v"(xop + 4 * 1280) : "memory");
      if constexpr (NN == 3)
        asm volatile("global_load_dwordx2 %0, %2, %3\n\tglobal_load_dwordx2 %1, %4, %3" : "=&v"(xs[0]), "=&v"(xs[1]) : "v"(xos), "s"(p.x), "v"(xos + 4 * 1280) : "memory");
    }
    if constexpr (D < 11) { halfD<NN, D + 1, 0>(acc, y, x, ad, mkseq<4 * NN>{}); waitl<0>(); }
    else halfD_last<NN>(acc, y, yp, ya, mkseq<4 * NN>{});
  });
  waitl<0>();
  LT();
  if constexpr (NN == 3) asm volatile("s_waitcnt vmcnt(0)" : "+v"(xp[0]), "+v"(xp[1]), "+v"(xs[0]), "+v"(xs[1])::"memory");
  else asm volatile("s_waitcnt vmcnt(0)" : "+v"(xp[0]), "+v"(xp[1])::"memory");
#pragma unroll
  for (int jj = 0; jj < 4; ++jj)
#pragma unroll
    for (int n = 0; n < NN; ++n) asm volatile("" : "+v"(yp[jj][n]));
  f32x2 ys[2][NN][2];
#pragma unroll
  for (int m = 0; m < 2; ++m)
#pragma unroll
    for (int n = 0; n < NN; ++n)
#pragma unroll
      for (int h = 0; h < 2; ++h)
        ys[m][n][h] = (bfpair(yp[0][n][m * 2 + h]) + bfpair(yp[1][n][m * 2 + h])) - (bfpair(yp[2][n][m * 2 + h]) + bfpair(yp[3][n][m * 2 + h]));
  sched_fence();
  mfma_settle();
  f32x2 o[2 * NN * 2];
#pragma unroll
  for (int m = 0; m < 2; ++m)
#pragma unroll
    for (int n = 0; n < NN; ++n) { o[(m * NN + n) * 2] = f32x2{acc[m][n][0], acc[m][n][1]}; o[(m * NN + n) * 2 + 1] = f32x2{acc[m][n][2], acc[m][n][3]}; }
  silu_pairs<2 * NN * 2>(o);
#pragma unroll
  for (int m = 0; m < 2; ++m) {
    u32x4 r;
#pragma unroll
    for (int n = 0; n < 2; ++n)
#pragma unroll
      for (int h = 0; h < 2; ++h) {
        f32x2 t = bfpair(xp[m][n * 2 + h]) + ys[m][n][h] * o[(m * NN + n) * 2 + h];
        r[n * 2 + h] = pk2(t[0], t[1]);
      }
    asm volatile("global_store_dwordx4 %0, %1, %2\n\ts_nop 1" ::"v"(xop + m * 4 * 1280), "v"(r), "s"(p.out) : "memory");
    if constexpr (NN == 3) {
      u32x2 r2;
#pragma unroll
      for (int h = 0; h < 2; ++h) {
        f32x2 t = bfpair(xs[m][h]) + ys[m][2][h] * o[(m * NN + 2) * 2 + h];
        r2[h] = pk2(t[0], t[1]);
      }
      asm volatile("global_store_dwordx2 %0, %1, %2\n\ts_nop 1" ::"v"(xos + m * 4 * 1280), "v"(r2), "s"(p.out) : "memory");
    }
  }
  LT();
}

__global__ __launch_bounds__(512, 2) void fnet_fused(Params p) {
  __shared__ __attribute__((aligned(16))) char lds[L_SIZE];
  u64 rt0; asm volatile("s_memrealtime %0\n\ts_waitcnt lgkmcnt(0)" : "=s"(rt0));
  const int tid = threadIdx.x, lane = tid & 63;
  const int wid = __builtin_amdgcn_readfirstlane(tid >> 6);
  const int blk = blockIdx.x, xcd = blk & 7, idx = blk >> 3;
  const int rb1 = (xcd & 1) * 16 + (idx >> 1), cb1 = (xcd >> 1) * 2 + (idx & 1);
  const int j = xcd & 3, rb2 = (xcd >> 2) * 8 + (idx >> 2), cb2 = idx & 3;
  const unsigned tok = 1;
  asm volatile("" ::"v"((unsigned)(size_t)lds));
  DIAG_DECL;
  RT(); LT();

  if (wid >= 4) {
    const int lw = wid - 4;
    unsigned dummy = 0;
    {
      const char* hb = ((xcd >> 1) < 2 ? p.hr : p.hi) + (size_t)((xcd >> 1) & 1) * 320 * 1280;
      touchs(dummy, (idx * 100 + lw * 25 + (lane < 25 ? lane : 24)) * 128, hb);
    }
    unsigned va[5];
#pragma unroll
    for (int u = 0; u < 5; ++u) {
      const int row = lw * 40 + u * 8 + (lane >> 3);
      const int key = (row >> 5) == 2 ? ((row >> 1) & 7) : (((row >> 1) & 1) | (((row >> 3) & 3) << 1));
      va[u] = row * 1280 + (((lane & 7) ^ key) << 4) - (u < 4 ? u * 1024 : 0);
    }
    const char* ha = (cb1 < 4 ? p.hr : p.hi) + (size_t)(cb1 & 3) * 160 * 1280;
    auto issA = [&](int t) {
      const unsigned m = L_R + (t & 3) * 20480 + lw * 5120;
      dma4<false>(m, va[0], va[1], va[2], va[3], ha + t * 128);
      dma1<false>(m + 4096, va[4], ha + t * 128);
    };
    unsigned vb[3];
#pragma unroll
    for (int u = 0; u < 3; ++u) {
      const int G = u * 64 + lane, row = lw * 8 + G / 24, c = G % 24;
      const int g = ((row >> 1) & 1) | (((row >> 3) & 1) << 1);
      vb[u] = row * 3072 + ((c ^ (g << 1)) << 4) - u * 1024;
    }
    const char* wb = p.w1 + cb1 * 384;
    auto issW = [&](int u, int ks) { dma3<false>(w1slot(u, ks) + lw * 3072, vb[0], vb[1], vb[2], wb + (size_t)(u * 64 + ks * 32) * 3072); };
    const int ep = EP0 + (((idx >> 1) * EPN) >> 2);
    const char* tb = (j < 2 ? p.cr : p.ci) + (size_t)((xcd >> 2) * 1024 + (idx & 1) * 512 + lw * 128) * 4096 + (j & 1) * 2048 + (((idx >> 1) + ROT2 * ((idx & 1) * 4 + lw)) & 15) * 128;
    const unsigned tvo = lane * 4096;
    auto step = [&](int n) {
      touch2_if(dummy, n, ep, tvo, tb, tb + 64 * 4096);
      LT();
    };
    const int pb = blk < 240 ? blk : blk - 16;
    const char* w2p = p.w2 + __builtin_amdgcn_readfirstlane((pb / 5) * 32 * 1280 + (pb % 5) * 256);
    asm volatile("s_waitcnt vmcnt(0)" : "+v"(dummy)::"memory");
    LT();
    issA(0); issA(1); issA(2); issA(3);
    {
      const int n = idx * 120 + lw * 30 + (lane < 30 ? lane : 29);
      const int l2 = lane < 48 ? lane : 47;
      const int r = lw * 8 + ((l2 - 32) >> 1);
      const char* a = lane < 32 ? p.w1 + (size_t)(n / 6) * 3072 + (xcd >> 1) * 768 + (n % 6) * 128
                                : w2p + r * 1280 + (l2 & 1) * 128;
      touch(dummy, a);
    }
    waitv<16>();
    bar(); step(1);
    waitv<10>(); bar(); issA(4); step(2);
    waitv<10>(); bar(); issA(5); step(3);
    waitv<10>(); bar(); issA(6); step(4);
    waitv<10>(); bar(); issA(7); step(5);
    waitv<10>(); bar(); issA(8); step(6);
    waitv<10>(); bar(); issA(9); step(7);
    waitv<10>(); bar(); issW(0, 0); step(8);
    waitv<8>();  bar(); issW(0, 1); issW(1, 0); step(9);
    waitv<9>();  bar(); issW(1, 1); step(10);
    waitv<6>();  bar(); issW(2, 0); issW(2, 1); step(11);
    unsigned vc[4];
#pragma unroll
    for (int u = 0; u < 4; ++u) {
      const int row = 8 * u + (lane >> 3), key = ((row >> 1) & 1) | (u << 1);
      vc[u] = row * 4096 + (((lane & 7) ^ key) << 4) - u * 1024;
    }
    const char* ca = (j < 2 ? p.cr : p.ci) + (size_t)(rb2 * 128 + lw * 32) * 4096 + (j & 1) * 2048;
    const unsigned vl = lane * 16;
    const char* cb = p.ws + WS_AB + (size_t)(j * 32 * 40 + cb2 * 10) * 1024 + (lw >> 1) * 40960 + (lw & 1) * 5120;
    const int rot2 = ROT2 * (idx >> 2);
    auto issCA = [&](int t) { dma4<false>(L_AR + ((t + 2) & 3) * 16384 + lw * 4096, vc[0], vc[1], vc[2], vc[3], ca + ((t + rot2) & 15) * 128); };
    auto issCB = [&](int t) {
      const unsigned m = L_R + (t & 3) * 20480 + lw * 5120;
      const char* s = cb + (size_t)((t + rot2) & 15) * 81920;
      dma4<true>(m, vl, vl, vl, vl, s);
      dma1<true>(m + 4096, vl, s + 4096);
    };
    const unsigned fao = WS_FA + (((j >> 1) * 4 + cb2) * 32 + (j & 1) * 16 + (lane & 15)) * 4;
    unsigned fa1 = 0, fa2 = 0;
    waitv<6>();  bar(); issW(3, 0); issW(3, 1); step(12);
    waitv<6>();  bar(); issW(4, 0); issW(4, 1); issW(5, 0);
    {
      const unsigned vw = (lw * 8 + (lane >> 4)) * 1280 + (lane & 15) * 16;
      dma2<false>((lw < 2 ? L_SP0 : L_SP1 - 4096) + lw * 2048, vw, vw + 4 * 1280 - 1024, w2p);
    }
    step(13);
    waitv<11>(); bar(); issW(5, 1); issW(6, 0); issW(6, 1); step(14);
    waitv<14>(); bar(); issW(7, 0); issW(7, 1); step(15);
    waitv<12>(); bar(); issW(8, 0); issW(8, 1); step(16);
    waitv<12>(); bar(); issW(9, 0); issW(9, 1); step(17);
    waitv<12>(); bar(); issCA(0); step(18);
    waitv<10>(); bar(); issCA(1); poll_req(fa1, p.ws, fao); step(19);
    waitv<9>();  bar(); poll_req(fa2, p.ws, fao); step(20);
    bar();
    asm volatile("s_waitcnt vmcnt(1)" : "+v"(fa1)::"memory");
    if (__builtin_amdgcn_ballot_w64(fa1 != tok) != 0) {
      asm volatile("s_waitcnt vmcnt(0)" : "+v"(fa2)::"memory");
      poll_spin(fa2, p.ws, fao, tok);
    }
    issCB(0); issCB(1); issCA(2); issCB(2); issCA(3); issCB(3);
    step(21);
    waitv<23>(); bar(); step(22);
    const char* da = p.ws + WS_H + (size_t)(rb2 * 2 + (j >> 1)) * 48 * 4096 + lw * 4096;
    const char* db = p.ws + WS_W2 + (size_t)(cb2 * 2 + (j & 1)) * 48 * 5120 + lw * 5120;
    const int rot3 = ROT3 * (3 * cb2 + ((3 * (idx >> 2)) >> 1));
    auto issD = [&](int d) {
      const int dr = (d + rot3) % 12;
      dma4<true>(L_AR + ((d + 2) & 3) * 16384 + lw * 4096, vl, vl, vl, vl, da + (size_t)dr * 16384);
      const unsigned m = L_R + (d & 3) * 20480 + lw * 5120;
      const char* s = db + (size_t)dr * 20480;
      dma4<true>(m, vl, vl, vl, vl, s);
      dma1<true>(m + 4096, vl, s + 4096);
    };
    u32x4 fb = {0, 0, 0, 0};
    const unsigned fbo = WS_FB + lane * 16;
    sfor<12>([&](auto CC) {
      constexpr int c = CC.value;
      waitv<18>(); bar(); issCA(c + 4); issCB(c + 4);
      if constexpr (c == 9) poll_req4(fb, p.ws, fbo);
      step(23 + c);
    });
    waitv<18>(); bar();
    poll_spin4(fb, p.ws, fbo, tok);
    issD(0); LT();
    waitv<18>(); bar(); issD(1); LT();
    waitv<18>(); bar(); issD(2); LT();
    waitv<18>(); bar(); issD(3); LT();
    const int tile = rb2 * 4 + cb2;
    unsigned fc = 0;
    const unsigned fco = WS_FC + (tile * 4 + (lane & 3)) * 4;
    sfor<8>([&](auto DD) {
      constexpr int d = DD.value;
      waitv<18>(); bar(); issD(d + 4);
      if constexpr (d == 6) poll_req(fc, p.ws, fco);
      LT();
    });
    waitv<18>(); bar();
    asm volatile("s_waitcnt vmcnt(9)" : "+v"(fc)::"memory");
    poll_spin(fc, p.ws, fco, tok);
    if (lw < 3) {
      const char* s = p.ws + WS_YP + (size_t)((tile * 4 + j) * 4 + ((j + 1 + lw) & 3)) * 10240;
      const unsigned m = lw == 0 ? L_R : lw == 1 ? L_R + 10240 : 32768u;
      dma4<true>(m, vl, vl, vl, vl, s);
      dma4<true>(m + 4096, vl, vl, vl, vl, s + 4096);
      dma2<true>(m + 8192, vl, vl, s + 8192);
    }
    LT();
    bar(); LT();
    if (lw < 3) waitv<10>(); else waitv<0>();
    bar(); LT();
    waitv<0>(); bar(); LT();
    asm volatile("" : "+v"(dummy));
    DIAG_OUT();
    for (int n = 0; n < 100000; ++n) { u64 t; asm volatile("s_sleep 2\n\ts_memrealtime %0\n\ts_waitcnt lgkmcnt(0)" : "=s"(t)); if (t - rt0 >= SPINT) break; }
  } else {
    const int cw = wid, wm = cw >> 1, wn = cw & 1;
    __builtin_amdgcn_s_setprio(3);
    {
      u32x4 xr[2][10];
      const unsigned xo = (rb1 * 64 + cw * 16 + (lane >> 3)) * 1280 + (lane & 7) * 16;
#define XLOAD(B, O)                                                                                                                   \
      asm volatile(                                                                                                                   \
          "global_load_dwordx4 %0, %10, %11\n\tglobal_load_dwordx4 %1, %10, %11 offset:128\n\t"                                       \
          "global_load_dwordx4 %2, %10, %11 offset:256\n\tglobal_load_dwordx4 %3, %10, %11 offset:384\n\t"                            \
          "global_load_dwordx4 %4, %10, %11 offset:512\n\tglobal_load_dwordx4 %5, %10, %11 offset:640\n\t"                            \
          "global_load_dwordx4 %6, %10, %11 offset:768\n\tglobal_load_dwordx4 %7, %10, %11 offset:896\n\t"                            \
          "global_load_dwordx4 %8, %10, %11 offset:1024\n\tglobal_load_dwordx4 %9, %10, %11 offset:1152"                              \
          : "=&v"(xr[B][0]), "=&v"(xr[B][1]), "=&v"(xr[B][2]), "=&v"(xr[B][3]), "=&v"(xr[B][4]), "=&v"(xr[B][5]), "=&v"(xr[B][6]),    \
            "=&v"(xr[B][7]), "=&v"(xr[B][8]), "=&v"(xr[B][9])                                                                         \
          : "v"(O), "s"(p.x) : "memory")
      XLOAD(0, xo);
      XLOAD(1, xo + 8 * 1280);
      sfor<2>([&](auto BB) {
        constexpr int B = BB.value;
        f32x2 v[40];
        f32x2 s = {0.f, 0.f};
        sfor<10>([&](auto KK) {
          constexpr int k = KK.value;
          asm volatile("s_waitcnt vmcnt(%2)" : "+v"(xr[B][k]), "+v"(s) : "i"(19 - B * 10 - k) : "memory");
#pragma unroll
          for (int e = 0; e < 4; ++e) { v[k * 4 + e] = bfpair(xr[B][k][e]); s += v[k * 4 + e]; }
        });
        if constexpr (B == 0) LT();
        const float mean = red8(s[0] + s[1]) * (1.0f / 640.0f);
        f32x2 s2a = {0.f, 0.f}, s2b = {0.f, 0.f};
#pragma unroll
        for (int k = 0; k < 40; k += 2) {
          v[k] -= f32x2{mean, mean}; s2a += v[k] * v[k];
          v[k + 1] -= f32x2{mean, mean}; s2b += v[k + 1] * v[k + 1];
        }
        s2a += s2b;
        const float rstd = __builtin_amdgcn_rsqf(red8(s2a[0] + s2a[1]) * (1.0f / 640.0f) + 1e-5f);
        const int row = cw * 16 + B * 8 + (lane >> 3);
        const int key = ((row >> 1) & 1) | (((row >> 3) & 3) << 1);
        const unsigned a = L_XN + row * 128 + (((lane & 7) ^ key) << 4);
        const unsigned ah = a + 65536;
        sfor<10>([&](auto KK) {
          constexpr int k = KK.value;
          u32x4 o;
#pragma unroll
          for (int e = 0; e < 4; ++e) { f32x2 t = v[k * 4 + e] * f32x2{rstd, rstd}; o[e] = pk2(t[0], t[1]); }
          if constexpr (k < 8) dsw<k * 8192>(a, o); else dsw<(k - 8) * 8192>(ah, o);
        });
      });
    }
    const int i = lane & 15, q = lane >> 4;
    AdX ax; AdA aa;
    {
      const int key2 = ((i >> 1) & 1) | ((i >> 2) << 1);
      const int r0 = wm * 32 + 8 * (i >> 2) + (i & 3);
      ax.a0 = L_XN + r0 * 128 + ((q ^ key2) << 4); ax.a1 = ax.a0 ^ 64;
      ax.a0h = ax.a0 + 65536; ax.a1h = ax.a1 + 65536;
      aa.p0 = L_R + ((wn ? 96 : 0) + 8 * (i >> 2) + (i & 3)) * 128 + ((q ^ key2) << 4); aa.p1 = aa.p0 ^ 64;
      aa.p0s = aa.p0 + 3 * 20480; aa.p1s = aa.p1 + 3 * 20480;
      aa.s0 = L_R + ((wn ? 80 : 64) + i) * 128 + ((q ^ (i >> 1)) << 4); aa.s1 = aa.s0 ^ 64;
      asm volatile("" : "+v"(ax.a0), "+v"(ax.a1), "+v"(ax.a0h), "+v"(ax.a1h));
      asm volatile("" : "+v"(aa.p0), "+v"(aa.p1), "+v"(aa.p0s), "+v"(aa.p1s), "+v"(aa.s0), "+v"(aa.s1));
    }
    const int gq = lane >> 4, qq = (lane >> 2) & 3, pp = lane & 3;
    AdB ab;
    {
      const int g = ((qq >> 1) & 1) | ((gq & 1) << 1);
#pragma unroll
      for (int n = 0; n < 4; ++n) {
        ab.e[n] = (8 * gq + qq) * 384 + (((6 * wn + n) ^ g) << 5) + pp * 8;
        ab.r[n] = ab.e[n] + L_R; ab.r2[n] = ab.r[n] + 65536;
        asm volatile("" : "+v"(ab.e[n]), "+v"(ab.r[n]), "+v"(ab.r2[n]));
      }
    }
    waitl<0>();
    LT();
    bar();
    LT();
    FrB xb, yb;
    {
      f32x4 acc[2][5];
      FrA x, y;
      rdA_first(x, ax, aa, mkseq<7>{});
      waitl<0>();
      sfor<10>([&](auto TT) {
        constexpr int T = TT.value;
        halfA<T, 1, T == 0>(acc, x, y, ax, aa, mkseq<10>{});
        waitl<0>(); bar();
        if constexpr (T < 9) halfA<T + 1, 0, false>(acc, y, x, ax, aa, mkseq<10>{});
        else halfA_last(acc, y, xb, ax, ab, mkseq<10>{});
        waitl<0>();
      });
      LT();
      mfma_settle();
      unsigned ao = WS_AB + (((cb1 >> 2) * 64 + rb1 * 2 + wm) * 40 + (cb1 & 3) * 10 + wn * 5) * 1024 + lane * 16;
      sfor<5>([&](auto NN_) {
        constexpr int n = NN_.value;
        u32x4 o = {pk2(acc[0][n][0], acc[0][n][1]), pk2(acc[0][n][2], acc[0][n][3]), pk2(acc[1][n][0], acc[1][n][1]), pk2(acc[1][n][2], acc[1][n][3])};
        if constexpr (n == 4) { ao += 4096; asm volatile("" : "+v"(ao)); }
        gst16<(n & 3) * 1024>(p.ws, ao, o);
      });
      LT();
    }
    {
      f32x4 acc[2][6];
      sfor<10>([&](auto UU) {
        constexpr int U = UU.value;
        halfB<U, 1, true, U == 0>(acc, xb, yb, ax, ab, mkseq<12>{});
        waitl<0>();
        if constexpr (U == 4) waitv<0>();
        bar();
        if constexpr (U == 4) {
          if (cw == 0 && lane == 0) gst4(p.ws, WS_FA + (cb1 * 32 + rb1) * 4, tok);
          const int B5 = (blk % 5) * 4 + cw, r5 = B5 % 5, dg = B5 / 5;
          const bool single = r5 == 2;
          const unsigned ta = L_SP0 + (4 * gq + qq) * 256 + cw * 64 + (single ? pp * 8 : pp * 16);
          const unsigned tb2 = ta + (single ? 32 : 8);
          bf16x4 t[2][2];
          dstr<0>(t[0][0], ta); dstr<L_SP1 - L_SP0>(t[0][1], ta); dstr<0>(t[1][0], tb2); dstr<L_SP1 - L_SP0>(t[1][1], tb2);
          asm volatile("s_waitcnt lgkmcnt(0)" : "+v"(t[0][0]), "+v"(t[0][1]), "+v"(t[1][0]), "+v"(t[1][1]));
#pragma unroll
          for (int b = 0; b < 2; ++b) {
            const int qg = 2 * dg + (r5 > 2 ? 1 : (single ? b : 0));
            const int n = single ? 4 : ((r5 == 1 || r5 == 4) ? 2 + b : b);
            const unsigned wo = (blk < 240 ? WS_W2 + ((qg * 48 + blk / 5) * 5 + n) * 1024 : WS_DUM + (cw * 2 + b) * 1024) + lane * 16;
            gst16<0>(p.ws, wo, __builtin_bit_cast(u32x4, cat(t[b][0], t[b][1])));
          }
        }
        halfB<U + 1, 0, (U < 9), false>(acc, yb, xb, ax, ab, mkseq<12>{});
        if constexpr (U < 9) waitl<0>();
      });
      LT();
      mfma_settle();
      const unsigned ho = WS_H + ((rb1 * 48 + cb1 * 6 + wn * 3) * 4 + wm * 2) * 1024 + lane * 16;
      sfor<2>([&](auto MM) {
        constexpr int m = MM.value;
        f32x2 v[12];
#pragma unroll
        for (int n = 0; n < 6; ++n) { v[2 * n] = f32x2{acc[m][n][0], acc[m][n][1]}; v[2 * n + 1] = f32x2{acc[m][n][2], acc[m][n][3]}; }
        silu_pairs<12>(v);
#pragma unroll
        for (int pr = 0; pr < 3; ++pr) {
          u32x4 o = {pk2(v[4 * pr][0], v[4 * pr][1]), pk2(v[4 * pr + 1][0], v[4 * pr + 1][1]), pk2(v[4 * pr + 2][0], v[4 * pr + 2][1]), pk2(v[4 * pr + 3][0], v[4 * pr + 3][1])};
          gst16<m * 1024>(p.ws, ho + pr * 4096, o);
        }
      });
      LT();
    }
    if (wn == 0) stage23<3>(p, cw, lane, blk, wid, tok, rb2, cb2, j DIAG_PASS);
    else stage23<2>(p, cw, lane, blk, wid, tok, rb2, cb2, j DIAG_PASS);
    DIAG_OUT();
    for (int n = 0; n < 100000; ++n) { u64 t; asm volatile("s_sleep 2\n\ts_memrealtime %0\n\ts_waitcnt lgkmcnt(0)" : "=s"(t)); if (t - rt0 >= SPINT) break; }
  }
}

extern "C" void kernel_launch(void* const* d_in, const int* in_sizes, int n_in, void* d_out, int out_size, void* d_ws, size_t ws_size,
                              hipStream_t stream) {
  Params p;
  p.x = (const char*)d_in[0]; p.cr = (const char*)d_in[1]; p.ci = (const char*)d_in[2];
  p.hr = (const char*)d_in[3]; p.hi = (const char*)d_in[4]; p.w1 = (const char*)d_in[6]; p.w2 = (const char*)d_in[7];
  p.out = (char*)d_out; p.ws = (char*)d_ws;

  fnet_fused<<<256, 512, 0, stream>>>(p);
}
```

```cpp
#include <hip/hip_runtime.h>
#include <stdint.h>
#include <utility>

#define DI __device__ __forceinline__
typedef short bf16x8 __attribute__((ext_vector_type(8)));
typedef short bf16x4 __attribute__((ext_vector_type(4)));
typedef float f32x4 __attribute__((ext_vector_type(4)));
typedef float f32x2 __attribute__((ext_vector_type(2)));
typedef unsigned u32x4 __attribute__((ext_vector_type(4)));
typedef unsigned u32x2 __attribute__((ext_vector_type(2)));
typedef unsigned long long u64;
template <int... I> using iseq = std::integer_sequence<int, I...>;
template <int N> using mkseq = std::make_integer_sequence<int, N>;
template <int N> struct IC { static constexpr int value = N; };
template <class F, int... I> DI void sfor_impl(F&& f, iseq<I...>) { (f(IC<I>{}), ...); }
template <int N, class F> DI void sfor(F&& f) { sfor_impl(f, mkseq<N>{}); }

struct Params {
  const char* x; const char* cr; const char* ci; const char* hr; const char* hi;
  const char* w1; const char* w2; char* out; char* ws;
};

constexpr unsigned WS_AB = 0;
constexpr unsigned WS_H  = WS_AB + 5242880;
constexpr unsigned WS_W2 = WS_H + 6291456;
constexpr unsigned WS_YP = WS_W2 + 1966080;
constexpr unsigned WS_DUM = WS_YP + 10485760;
constexpr unsigned WS_FA = WS_DUM + 8192;
constexpr unsigned WS_FB = WS_FA + 1024;
constexpr unsigned WS_FC = WS_FB + 1024;
constexpr unsigned WS_END = WS_FC + 1024;

constexpr unsigned L_XN = 0;
constexpr unsigned L_R = 81920;
constexpr unsigned L_AR = 0;
constexpr unsigned L_OWN = 65536;
constexpr unsigned L_SP0 = L_R + 36864, L_SP1 = L_R + 77824;
constexpr unsigned L_SIZE = 163840;
constexpr unsigned w1slot(int u, int ks) {
  constexpr unsigned H0 = L_R + 40960, H1 = L_R + 53248, H2 = L_R + 65536, H3 = L_R, H4 = L_R + 12288, H5 = L_R + 24576, E0 = 0, E1 = 12288;
  switch (u) {
    case 0: case 3: case 7: return ks ? H1 : H0;
    case 1: case 4: case 8: return ks ? H3 : H2;
    case 2: case 6: return ks ? H5 : H4;
    default: return ks ? E1 : E0;
  }
}

#define EP0 1
#define EPN 6
#define ROT2 2
#define ROT3 1
#define SPIN_MAX 2000000

template <int OFF> DI void dsr(bf16x8& d, unsigned a) { static_assert(OFF >= 0 && OFF < 65536, ""); asm volatile("ds_read_b128 %0, %1 offset:%2" : "=v"(d) : "v"(a), "i"(OFF)); }
template <int OFF> DI void dsr4(u32x4& d, unsigned a) { static_assert(OFF >= 0 && OFF < 65536, ""); asm volatile("ds_read_b128 %0, %1 offset:%2" : "=v"(d) : "v"(a), "i"(OFF)); }
template <int OFF> DI void dstr(bf16x4& d, unsigned a) { static_assert(OFF >= 0 && OFF < 65536, ""); asm volatile("ds_read_b64_tr_b16 %0, %1 offset:%2" : "=v"(d) : "v"(a), "i"(OFF)); }
template <int OFF> DI void dsw(unsigned a, const u32x4& d) { static_assert(OFF >= 0 && OFF < 65536, ""); asm volatile("ds_write_b128 %0, %1 offset:%2" ::"v"(a), "v"(d), "i"(OFF) : "memory"); }
template <bool Z> DI void mfma(f32x4& c, const bf16x8& a, const bf16x8& b) {
  if constexpr (Z) asm volatile("v_mfma_f32_16x16x32_bf16 %0, %1, %2, 0" : "=&v"(c) : "v"(a), "v"(b));
  else asm volatile("v_mfma_f32_16x16x32_bf16 %0, %1, %2, %0" : "+v"(c) : "v"(a), "v"(b));
}
template <int N> DI void waitl() { static_assert(N >= 0 && N < 16, ""); __builtin_amdgcn_s_waitcnt(0xC07F | (N << 8)); }
template <int N> DI void waitv() { static_assert(N >= 0 && N < 64, ""); asm volatile("s_waitcnt vmcnt(%0)" ::"i"(N) : "memory"); }
DI void bar() { asm volatile("s_barrier" ::: "memory"); }
DI void mfma_settle() { asm volatile("s_nop 7\n\ts_nop 7" ::: "memory"); }
DI void sched_fence() { __builtin_amdgcn_sched_barrier(0); }

#define DMA_HEAD "s_mov_b32 m0, %0\n\ts_nop 0\n\t"
#define DMA4(POL)                                                                                         \
  asm volatile(DMA_HEAD "global_load_lds_dwordx4 %1, %5 " POL "\n\t"                                      \
               "global_load_lds_dwordx4 %2, %5 offset:1024 " POL "\n\t"                                   \
               "global_load_lds_dwordx4 %3, %5 offset:2048 " POL "\n\t"                                   \
               "global_load_lds_dwordx4 %4, %5 offset:3072 " POL ::"s"(m), "v"(v0), "v"(v1), "v"(v2), "v"(v3), "s"(s) : "memory")
template <bool SC1> DI void dma4(unsigned m, unsigned v0, unsigned v1, unsigned v2, unsigned v3, const char* s) {
  if constexpr (SC1) DMA4("sc1"); else DMA4("");
}
#define DMA3(POL)                                                                                         \
  asm volatile(DMA_HEAD "global_load_lds_dwordx4 %1, %4 " POL "\n\t"                                      \
               "global_load_lds_dwordx4 %2, %4 offset:1024 " POL "\n\t"                                   \
               "global_load_lds_dwordx4 %3, %4 offset:2048 " POL ::"s"(m), "v"(v0), "v"(v1), "v"(v2), "s"(s) : "memory")
template <bool SC1> DI void dma3(unsigned m, unsigned v0, unsigned v1, unsigned v2, const char* s) {
  if constexpr (SC1) DMA3("sc1"); else DMA3("");
}
#define DMA2(POL)                                                                                         \
  asm volatile(DMA_HEAD "global_load_lds_dwordx4 %1, %3 " POL "\n\t"                                      \
               "global_load_lds_dwordx4 %2, %3 offset:1024 " POL ::"s"(m), "v"(v0), "v"(v1), "s"(s) : "memory")
template <bool SC1> DI void dma2(unsigned m, unsigned v0, unsigned v1, const char* s) {
  if constexpr (SC1) DMA2("sc1"); else DMA2("");
}
#define DMA1(POL) asm volatile(DMA_HEAD "global_load_lds_dwordx4 %1, %2 " POL ::"s"(m), "v"(v0), "s"(s) : "memory")
template <bool SC1> DI void dma1(unsigned m, unsigned v0, const char* s) {
  if constexpr (SC1) DMA1("sc1"); else DMA1("");
}

template <int OFF> DI void gst16(const char* base, unsigned voff, const u32x4& d) {
  static_assert(OFF >= 0 && OFF < 4096, "");
  asm volatile("global_store_dwordx4 %0, %1, %2 offset:%3 sc1\n\ts_nop 1" ::"v"(voff), "v"(d), "s"(base), "i"(OFF) : "memory");
}
DI void gst4(const char* base, unsigned voff, unsigned d) {
  asm volatile("global_store_dword %0, %1, %2 sc1" ::"v"(voff), "v"(d), "s"(base) : "memory");
}
DI unsigned pk2(float lo, float hi) { unsigned r; asm("v_cvt_pk_bf16_f32 %0, %1, %2" : "=v"(r) : "v"(lo), "v"(hi)); return r; }
DI float bflo(unsigned v) { return __uint_as_float(v << 16); }
DI float bfhi(unsigned v) { return __uint_as_float(v & 0xffff0000u); }
DI f32x2 bfpair(unsigned v) { return f32x2{bflo(v), bfhi(v)}; }
template <int CTRL> DI float dppadd(float v) {
  return v + __int_as_float(__builtin_amdgcn_update_dpp(0, __float_as_int(v), CTRL, 0xf, 0xf, true));
}
DI float red8(float v) { v = dppadd<0xB1>(v); v = dppadd<0x4E>(v); return dppadd<0x141>(v); }
template <int N> DI void silu_pairs(f32x2 (&v)[N]) {
  f32x2 t[N];
#pragma unroll
  for (int k = 0; k < N; ++k) t[k] = v[k] * f32x2{-1.44269504088896341f, -1.44269504088896341f};
  sched_fence();
#pragma unroll
  for (int k = 0; k < N; ++k) { t[k][0] = __builtin_amdgcn_exp2f(t[k][0]); t[k][1] = __builtin_amdgcn_exp2f(t[k][1]); }
  sched_fence();
#pragma unroll
  for (int k = 0; k < N; ++k) t[k] += f32x2{1.0f, 1.0f};
  sched_fence();
#pragma unroll
  for (int k = 0; k < N; ++k) { t[k][0] = __builtin_amdgcn_rcpf(t[k][0]); t[k][1] = __builtin_amdgcn_rcpf(t[k][1]); }
  sched_fence();
#pragma unroll
  for (int k = 0; k < N; ++k) v[k] *= t[k];
  sched_fence();
}

DI void touch(unsigned& d, const char* a) { asm volatile("global_load_dword %0, %1, off" : "+v"(d) : "v"(a) : "memory"); }
DI void touchs(unsigned& d, unsigned voff, const char* base) { asm volatile("global_load_dword %0, %1, %2" : "+v"(d) : "v"(voff), "s"(base) : "memory"); }
DI void touch2_if(unsigned& d, int n, int ep, unsigned voff, const char* b0, const char* b1) {
  asm volatile("s_cmp_lg_u32 %1, %2\n\ts_cbranch_scc1 .Lskip_touch%=\n\t"
               "global_load_dword %0, %3, %4\n\tglobal_load_dword %0, %3, %5\n"
               ".Lskip_touch%=:" : "+v"(d) : "s"(n), "s"(ep), "v"(voff), "s"(b0), "s"(b1) : "memory", "scc");
}

DI void poll_req(unsigned& fv, const char* base, unsigned voff) {
  asm volatile("global_load_dword %0, %1, %2 sc1" : "+v"(fv) : "v"(voff), "s"(base) : "memory");
}
DI void poll_req4(u32x4& fv, const char* base, unsigned voff) {
  asm volatile("global_load_dwordx4 %0, %1, %2 sc1" : "+v"(fv) : "v"(voff), "s"(base) : "memory");
}
DI void poll_spin(unsigned& fv, const char* base, unsigned voff, unsigned tok) {
  asm volatile("" : "+v"(fv));
  for (int n = 0; __builtin_amdgcn_ballot_w64(fv != tok) != 0 && n < SPIN_MAX; ++n)
    asm volatile("global_load_dword %0, %1, %2 sc1\n\ts_waitcnt vmcnt(0)" : "=v"(fv) : "v"(voff), "s"(base) : "memory");
}
DI void poll_spin4(u32x4& fv, const char* base, unsigned voff, unsigned tok) {
  asm volatile("" : "+v"(fv));
  for (int n = 0; __builtin_amdgcn_ballot_w64(fv[0] != tok || fv[1] != tok || fv[2] != tok || fv[3] != tok) != 0 && n < SPIN_MAX; ++n)
    asm volatile("global_load_dwordx4 %0, %1, %2 sc1\n\ts_waitcnt vmcnt(0)" : "=v"(fv) : "v"(voff), "s"(base) : "memory");
}

#define LT() do {} while (0)
#define RT() do {} while (0)
#define DIAG_DECL do {} while (0)
#define DIAG_OUT() do {} while (0)
#define DIAG_ARGS
#define DIAG_PASS

struct FrB { bf16x8 a[2]; bf16x4 w[6][2]; };
struct AdX { unsigned a0, a1, a0h, a1h; };
struct AdB { unsigned e[4], r[4], r2[4]; };
template <int I, int T, int KS> DI void rdX(bf16x8& d, const AdX& ad) {
  dsr<(T & 7) * 8192 + I * 512>(d, T < 8 ? (KS ? ad.a1 : ad.a0) : (KS ? ad.a1h : ad.a0h));
}
template <int I, int U, int KS> DI void rdB(FrB& f, const AdX& ax, const AdB& ad) {
  if constexpr (I < 2) rdX<I, U, KS>(f.a[I], ax);
  else {
    constexpr int n = (I - 2) >> 1, h = (I - 2) & 1;
    constexpr unsigned base = w1slot(U, KS), in = h * 1536 + (n >> 2) * 128;
    if constexpr (base < L_R) dstr<base + in>(f.w[n][h], ad.e[n & 3]);
    else if constexpr (base - L_R < 65536) dstr<base - L_R + in>(f.w[n][h], ad.r[n & 3]);
    else dstr<base - L_R - 65536 + in>(f.w[n][h], ad.r2[n & 3]);
  }
}

struct FrA { bf16x8 a[2], b[5]; };
struct AdA { unsigned p0, p1, p0s, p1s, s0, s1; };
template <int I, int T, int KS> DI void rdA(FrA& f, const AdX& ax, const AdA& ad) {
  if constexpr (I < 2) rdX<I, T, KS>(f.a[I], ax);
  else {
    constexpr int n = I - 2, s = T & 3;
    if constexpr (n == 4) dsr<s * 20480>(f.b[4], KS ? ad.s1 : ad.s0);
    else {
      constexpr int in = (n >> 1) * 4096 + (n & 1) * 512;
      if constexpr (s < 3) dsr<s * 20480 + in>(f.b[n], KS ? ad.p1 : ad.p0);
      else dsr<in>(f.b[n], KS ? ad.p1s : ad.p0s);
    }
  }
}
template <int G, int T, int KS> DI void gapA(FrA& y, const AdX& ax, const AdA& ad) {
  if constexpr (G < 4) { rdA<2 * G, T, KS>(y, ax, ad); if constexpr (2 * G + 1 < 7) rdA<2 * G + 1, T, KS>(y, ax, ad); }
}
template <int T, int KS, bool Z, int... G> DI void halfA(f32x4 (&acc)[2][5], FrA& x, FrA& y, const AdX& ax, const AdA& ad, iseq<G...>) {
  ((mfma<Z>(acc[G / 5][G % 5], x.a[G / 5], x.b[G % 5]), gapA<G, T, KS>(y, ax, ad)), ...);
}
template <int G> DI void gapAB(FrB& xb, const AdX& ax, const AdB& ab) {
  if constexpr (G < 7) { rdB<2 * G, 0, 0>(xb, ax, ab); rdB<2 * G + 1, 0, 0>(xb, ax, ab); }
}
template <int... G> DI void halfA_last(f32x4 (&acc)[2][5], FrA& y, FrB& xb, const AdX& ax, const AdB& ab, iseq<G...>) {
  ((mfma<false>(acc[G / 5][G % 5], y.a[G / 5], y.b[G % 5]), gapAB<G>(xb, ax, ab)), ...);
}
template <int... I> DI void rdA_first(FrA& x, const AdX& ax, const AdA& ad, iseq<I...>) { (rdA<I, 0, 0>(x, ax, ad), ...); }

template <int G, int U, int KS, bool RD> DI void gapB(FrB& y, const AdX& ax, const AdB& ad) {
  if constexpr (RD && G < 7) { rdB<2 * G, U, KS>(y, ax, ad); rdB<2 * G + 1, U, KS>(y, ax, ad); }
}
DI bf16x8 cat(const bf16x4& l, const bf16x4& h) { return __builtin_shufflevector(l, h, 0, 1, 2, 3, 4, 5, 6, 7); }
template <int U, int KS, bool RD, bool Z, int... G> DI void halfB(f32x4 (&acc)[2][6], FrB& x, FrB& y, const AdX& ax, const AdB& ad, iseq<G...>) {
  ((mfma<Z>(acc[G / 6][G % 6], cat(x.w[G % 6][0], x.w[G % 6][1]), x.a[G / 6]), gapB<G, U, KS, RD>(y, ax, ad)), ...);
}

struct FrC { bf16x8 a[4], b[5]; };
struct AdC { unsigned a0, a1, b0, b1; };
template <int I, int S, int KS> DI void rdC(FrC& f, const AdC& ad) {
  if constexpr (I >= 1 && I <= 5) {
    constexpr int n = I - 1, off = S * 20480 + KS * 10240 + n * 1024;
    if constexpr (S < 3) dsr<off>(f.b[n], ad.b0); else dsr<off - 3 * 20480>(f.b[n], ad.b1);
  } else {
    constexpr int m = (I == 0) ? 0 : I - 5, off = ((S + 2) & 3) * 16384 + (m >> 1) * 4096 + (m & 1) * 512;
    dsr<off>(f.a[m], KS ? ad.a1 : ad.a0);
  }
}
template <int G, int S> DI void gapC0(FrC& y, const AdC& ad) {
  if constexpr (G < 9) rdC<G, S, 1>(y, ad);
  if constexpr (G == 4) waitl<7>();
  if constexpr (G == 9) waitl<10>();
  if constexpr (G == 14) waitl<9>();
}
template <int G, int S> DI void gapC1(FrC& x, const AdC& ad) {
  if constexpr (G < 9) rdC<G, (S + 1) & 3, 0>(x, ad);
  if constexpr (G == 19) waitl<3>();
}
template <int S, int... G> DI void halfC0(f32x4 (&acc)[4][5], FrC& x, FrC& y, const AdC& ad, iseq<G...>) {
  ((mfma<false>(acc[G / 5][G % 5], x.b[G % 5], x.a[G / 5]), gapC0<G, S>(y, ad)), ...);
}
template <int S, int... G> DI void halfC1(f32x4 (&acc)[4][5], FrC& x, FrC& y, const AdC& ad, iseq<G...>) {
  ((mfma<false>(acc[G / 5][G % 5], y.b[G % 5], y.a[G / 5]), gapC1<G, S>(x, ad)), ...);
}
template <int... I> DI void rdC_first(FrC& x, const AdC& ad, iseq<I...>) { (rdC<I, 0, 0>(x, ad), ...); }

template <int NN> struct FrD { bf16x8 h[2][2], w[2][NN]; };
struct AdD { unsigned a, b, bs; };
template <int NN> constexpr int nfrag(int k) { return NN == 3 ? (k == 2 ? 4 : k) : 2 + k; }
template <int NN, int I, int D, int HF> DI void rdD(FrD<NN>& f, const AdD& ad) {
  constexpr int kk = I / (NN + 2), r = I % (NN + 2), ks = HF * 2 + kk;
  if constexpr (r < 2) dsr<((D + 2) & 3) * 16384 + ks * 4096 + r * 1024>(f.h[kk][r], ad.a);
  else if constexpr ((D & 3) < 3) dsr<(D & 3) * 20480 + ks * 5120 + nfrag<NN>(r - 2) * 1024>(f.w[kk][r - 2], ad.b);
  else dsr<ks * 5120 + nfrag<NN>(r - 2) * 1024>(f.w[kk][r - 2], ad.bs);
}
template <int NN, int G, int D, int HF> DI void gapD(FrD<NN>& y, const AdD& ad) {
  if constexpr (2 * G < 2 * (NN + 2)) rdD<NN, 2 * G, D, HF>(y, ad);
  if constexpr (2 * G + 1 < 2 * (NN + 2)) rdD<NN, 2 * G + 1, D, HF>(y, ad);
}
template <int NN, int G> DI void mfD(f32x4 (&acc)[2][NN], FrD<NN>& x) {
  constexpr int kk = G / (2 * NN), m = (G % (2 * NN)) / NN, n = G % NN;
  mfma<false>(acc[m][n], x.w[kk][n], x.h[kk][m]);
}
template <int NN, int D, int HF, int... G> DI void halfD(f32x4 (&acc)[2][NN], FrD<NN>& x, FrD<NN>& y, const AdD& ad, iseq<G...>) {
  ((mfD<NN, G>(acc, x), gapD<NN, G, D, HF>(y, ad)), ...);
}
template <int NN, int G> DI void gapCD(FrD<NN>& x, const AdD& ad) { if constexpr (G < 2 * (NN + 2)) rdD<NN, G, 0, 0>(x, ad); }
template <int NN, int... G> DI void halfC_last(f32x4 (&acc)[4][5], FrC& y, FrD<NN>& xd, const AdD& ad, iseq<G...>) {
  ((mfma<false>(acc[G / 5][G % 5], y.b[G % 5], y.a[G / 5]), gapCD<NN, G>(xd, ad)), ...);
}
template <int NN, int G> DI void gapY(u32x4 (&yp)[4][NN], const unsigned (&ya)[4]) {
  if constexpr (G < 2 * NN) {
    constexpr int i0 = 2 * G, i1 = 2 * G + 1;
    dsr4<nfrag<NN>(i0 % NN) * 1024>(yp[i0 / NN][i0 % NN], ya[i0 / NN]);
    dsr4<nfrag<NN>(i1 % NN) * 1024>(yp[i1 / NN][i1 % NN], ya[i1 / NN]);
  }
}
template <int NN, int... G> DI void halfD_last(f32x4 (&acc)[2][NN], FrD<NN>& y, u32x4 (&yp)[4][NN], const unsigned (&ya)[4], iseq<G...>) {
  ((mfD<NN, G>(acc, y), gapY<NN, G>(yp, ya)), ...);
}

template <int NN>
DI void stage23(const Params& p, int cw, int lane, int blk, int wid, unsigned tok, int rb2, int cb2, int j DIAG_ARGS) {
  const int wm3 = cw >> 1;
  const int tile = rb2 * 4 + cb2;
  const int i = lane & 15, q = lane >> 4, par = j & 1;
  f32x4 acc2[4][5];
  FrC xc, yc;
  {
    AdC ad;
    const int wm = cw >> 1, wn = NN == 3 ? 0 : 1;
    const int key2 = ((i >> 1) & 1) | ((i >> 2) << 1);
    const int r0 = wm * 64 + 8 * (i >> 2) + (i & 3);
    ad.a0 = L_AR + r0 * 128 + ((q ^ key2) << 4); ad.a1 = ad.a0 ^ 64;
    ad.b0 = L_R + wn * 5120 + lane * 16; ad.b1 = ad.b0 + 3 * 20480;
    asm volatile("" : "+v"(ad.a0), "+v"(ad.a1), "+v"(ad.b0), "+v"(ad.b1));
#pragma unroll
    for (int m = 0; m < 4; ++m)
#pragma unroll
      for (int n = 0; n < 5; ++n) { acc2[m][n] = f32x4{0, 0, 0, 0}; asm volatile("" : "+v"(acc2[m][n])); }
    bar();
    LT();
    rdC_first(xc, ad, mkseq<9>{});
    waitl<0>();
#pragma unroll 1
    for (int g = 0; g < 4; ++g) {
      halfC0<0>(acc2, xc, yc, ad, mkseq<20>{}); waitl<0>();
      if (g == 1) waitv<0>();
      bar();
      if (g == 1 && cw == 0 && lane == 0) gst4(p.ws, WS_FB + blk * 4, tok);
      halfC1<0>(acc2, xc, yc, ad, mkseq<20>{});
      halfC0<1>(acc2, xc, yc, ad, mkseq<20>{}); waitl<0>(); bar();
      halfC1<1>(acc2, xc, yc, ad, mkseq<20>{});
      halfC0<2>(acc2, xc, yc, ad, mkseq<20>{}); waitl<0>(); bar();
      halfC1<2>(acc2, xc, yc, ad, mkseq<20>{});
      halfC0<3>(acc2, xc, yc, ad, mkseq<20>{}); waitl<0>(); bar();
      if (g < 3) halfC1<3>(acc2, xc, yc, ad, mkseq<20>{});
    }
  }
  AdD ad;
  ad.a = L_AR + wm3 * 2048 + lane * 16;
  ad.b = L_R + lane * 16;
  ad.bs = ad.b + 3 * 20480;
  asm volatile("" : "+v"(ad.a), "+v"(ad.b), "+v"(ad.bs));
  FrD<NN> x, y;
  halfC_last<NN>(acc2, yc, x, ad, mkseq<20>{});
  LT();
  mfma_settle();
  if (cw == j) {
    const unsigned a = L_OWN + lane * 16;
    sfor<10>([&](auto K) {
      constexpr int pr = K.value / 5, n = K.value % 5;
      u32x4 o = {pk2(acc2[2 * pr][n][0], acc2[2 * pr][n][1]), pk2(acc2[2 * pr][n][2], acc2[2 * pr][n][3]),
                 pk2(acc2[2 * pr + 1][n][0], acc2[2 * pr + 1][n][1]), pk2(acc2[2 * pr + 1][n][2], acc2[2 * pr + 1][n][3])};
      dsw<K.value * 1024>(a, o);
    });
  } else {
    unsigned yo = WS_YP + ((tile * 4 + cw) * 4 + j) * 10240 + lane * 16;
    sfor<10>([&](auto K) {
      constexpr int pr = K.value / 5, n = K.value % 5;
      u32x4 o = {pk2(acc2[2 * pr][n][0], acc2[2 * pr][n][1]), pk2(acc2[2 * pr][n][2], acc2[2 * pr][n][3]),
                 pk2(acc2[2 * pr + 1][n][0], acc2[2 * pr + 1][n][1]), pk2(acc2[2 * pr + 1][n][2], acc2[2 * pr + 1][n][3])};
      if constexpr (K.value == 4 || K.value == 8) { yo += 4096; asm volatile("" : "+v"(yo)); }
      gst16<(K.value & 3) * 1024>(p.ws, yo, o);
    });
  }
  f32x4 acc[2][NN];
#pragma unroll
  for (int m = 0; m < 2; ++m)
#pragma unroll
    for (int n = 0; n < NN; ++n) { acc[m][n] = f32x4{0, 0, 0, 0}; asm volatile("" : "+v"(acc[m][n])); }
  waitl<0>();
  LT();
  const unsigned trow = rb2 * 128 + 64 * (j >> 1) + 32 * wm3 + 8 * (i >> 2) + (i & 3);
  const unsigned ecol = cb2 * 160 + 80 * par;
  const unsigned xop = trow * 1280 + (ecol + (NN == 3 ? (par ? 16 : 0) : (par ? 48 : 32))) * 2 + q * 16;
  const unsigned xos = trow * 1280 + (ecol + (par ? 0 : 64)) * 2 + q * 8;
  unsigned ya[4];
#pragma unroll
  for (int jj = 0; jj < 4; ++jj) {
    const int k = (jj - j - 1) & 3;
    ya[jj] = (k == 0 ? L_R : k == 1 ? L_R + 10240 : k == 2 ? 32768u : L_OWN) + wm3 * 5120 + lane * 16;
    asm volatile("" : "+v"(ya[jj]));
  }
  u32x4 xp[2];
  u32x2 xs[2];
  u32x4 yp[4][NN];
  sfor<12>([&](auto DD) {
    constexpr int D = DD.value;
    halfD<NN, D, 1>(acc, x, y, ad, mkseq<4 * NN>{});
    waitl<0>();
    if constexpr (D == 3) waitv<0>();
    bar();
    if constexpr (D == 3) { if (cw == 0 && lane == 0) gst4(p.ws, WS_FC + (tile * 4 + j) * 4, tok); }
    if constexpr (D == 8) {
      asm volatile("global_load_dwordx4 %0, %2, %3\n\tglobal_load_dwordx4 %1, %4, %3" : "=&v"(xp[0]), "=&v"(xp[1]) : "v"(xop), "s"(p.x), "v"(xop + 4 * 1280) : "memory");
      if constexpr (NN == 3)
        asm volatile("global_load_dwordx2 %0, %2, %3\n\tglobal_load_dwordx2 %1, %4, %3" : "=&v"(xs[0]), "=&v"(xs[1]) : "v"(xos), "s"(p.x), "v"(xos + 4 * 1280) : "memory");
    }
    if constexpr (D < 11) { halfD<NN, D + 1, 0>(acc, y, x, ad, mkseq<4 * NN>{}); waitl<0>(); }
    else halfD_last<NN>(acc, y, yp, ya, mkseq<4 * NN>{});
  });
  waitl<0>();
  LT();
  if constexpr (NN == 3) asm volatile("s_waitcnt vmcnt(0)" : "+v"(xp[0]), "+v"(xp[1]), "+v"(xs[0]), "+v"(xs[1])::"memory");
  else asm volatile("s_waitcnt vmcnt(0)" : "+v"(xp[0]), "+v"(xp[1])::"memory");
#pragma unroll
  for (int jj = 0; jj < 4; ++jj)
#pragma unroll
    for (int n = 0; n < NN; ++n) asm volatile("" : "+v"(yp[jj][n]));
  f32x2 ys[2][NN][2];
#pragma unroll
  for (int m = 0; m < 2; ++m)
#pragma unroll
    for (int n = 0; n < NN; ++n)
#pragma unroll
      for (int h = 0; h < 2; ++h)
        ys[m][n][h] = (bfpair(yp[0][n][m * 2 + h]) + bfpair(yp[1][n][m * 2 + h])) - (bfpair(yp[2][n][m * 2 + h]) + bfpair(yp[3][n][m * 2 + h]));
  sched_fence();
  mfma_settle();
  f32x2 o[2 * NN * 2];
#pragma unroll
  for (int m = 0; m < 2; ++m)
#pragma unroll
    for (int n = 0; n < NN; ++n) { o[(m * NN + n) * 2] = f32x2{acc[m][n][0], acc[m][n][1]}; o[(m * NN + n) * 2 + 1] = f32x2{acc[m][n][2], acc[m][n][3]}; }
  silu_pairs<2 * NN * 2>(o);
#pragma unroll
  for (int m = 0; m < 2; ++m) {
    u32x4 r;
#pragma unroll
    for (int n = 0; n < 2; ++n)
#pragma unroll
      for (int h = 0; h < 2; ++h) {
        f32x2 t = bfpair(xp[m][n * 2 + h]) + ys[m][n][h] * o[(m * NN + n) * 2 + h];
        r[n * 2 + h] = pk2(t[0], t[1]);
      }
    asm volatile("global_store_dwordx4 %0, %1, %2\n\ts_nop 1" ::"v"(xop + m * 4 * 1280), "v"(r), "s"(p.out) : "memory");
    if constexpr (NN == 3) {
      u32x2 r2;
#pragma unroll
      for (int h = 0; h < 2; ++h) {
        f32x2 t = bfpair(xs[m][h]) + ys[m][2][h] * o[(m * NN + 2) * 2 + h];
        r2[h] = pk2(t[0], t[1]);
      }
      asm volatile("global_store_dwordx2 %0, %1, %2\n\ts_nop 1" ::"v"(xos + m * 4 * 1280), "v"(r2), "s"(p.out) : "memory");
    }
  }
  LT();
}

__global__ __launch_bounds__(512, 2) void fnet_fused(Params p) {
  __shared__ __attribute__((aligned(16))) char lds[L_SIZE];
  const int tid = threadIdx.x, lane = tid & 63;
  const int wid = __builtin_amdgcn_readfirstlane(tid >> 6);
  const int blk = blockIdx.x, xcd = blk & 7, idx = blk >> 3;
  const int rb1 = (xcd & 1) * 16 + (idx >> 1), cb1 = (xcd >> 1) * 2 + (idx & 1);
  const int j = xcd & 3, rb2 = (xcd >> 2) * 8 + (idx >> 2), cb2 = idx & 3;
  const unsigned tok = 1;
  asm volatile("" ::"v"((unsigned)(size_t)lds));
  DIAG_DECL;
  RT(); LT();

  unsigned cdummy = 0;
  {
    u64 pc; unsigned len;
    asm volatile("s_getpc_b64 %0\n.Lfnet_pc:\n\ts_mov_b32 %1, .Lfnet_end-.Lfnet_pc" : "=s"(pc), "=s"(len));
    if (wid == 7) {
      unsigned off = (idx + 32 * lane) * 128;
      off = off < len - 4 ? off : len - 4;
      asm volatile("global_load_dword %0, %1, %2" : "+v"(cdummy) : "v"(off & ~3u), "s"(pc) : "memory");
    }
  }
  if (wid >= 4) {
    const int lw = wid - 4;
    unsigned dummy = 0;
    {
      const char* hb = ((xcd >> 1) < 2 ? p.hr : p.hi) + (size_t)((xcd >> 1) & 1) * 320 * 1280;
      touchs(dummy, (idx * 100 + lw * 25 + (lane < 25 ? lane : 24)) * 128, hb);
    }
    unsigned va[5];
#pragma unroll
    for (int u = 0; u < 5; ++u) {
      const int row = lw * 40 + u * 8 + (lane >> 3);
      const int key = (row >> 5) == 2 ? ((row >> 1) & 7) : (((row >> 1) & 1) | (((row >> 3) & 3) << 1));
      va[u] = row * 1280 + (((lane & 7) ^ key) << 4) - (u < 4 ? u * 1024 : 0);
    }
    const char* ha = (cb1 < 4 ? p.hr : p.hi) + (size_t)(cb1 & 3) * 160 * 1280;
    auto issA = [&](int t) {
      const unsigned m = L_R + (t & 3) * 20480 + lw * 5120;
      dma4<false>(m, va[0], va[1], va[2], va[3], ha + t * 128);
      dma1<false>(m + 4096, va[4], ha + t * 128);
    };
    unsigned vb[3];
#pragma unroll
    for (int u = 0; u < 3; ++u) {
      const int G = u * 64 + lane, row = lw * 8 + G / 24, c = G % 24;
      const int g = ((row >> 1) & 1) | (((row >> 3) & 1) << 1);
      vb[u] = row * 3072 + ((c ^ (g << 1)) << 4) - u * 1024;
    }
    const char* wb = p.w1 + cb1 * 384;
    auto issW = [&](int u, int ks) { dma3<false>(w1slot(u, ks) + lw * 3072, vb[0], vb[1], vb[2], wb + (size_t)(u * 64 + ks * 32) * 3072); };
    const int ep = EP0 + (((idx >> 1) * EPN) >> 2);
    const char* tb = (j < 2 ? p.cr : p.ci) + (size_t)((xcd >> 2) * 1024 + (idx & 1) * 512 + lw * 128) * 4096 + (j & 1) * 2048 + (((idx >> 1) + ROT2 * ((idx & 1) * 4 + lw)) & 15) * 128;
    const unsigned tvo = lane * 4096;
    auto step = [&](int n) {
      touch2_if(dummy, n, ep, tvo, tb, tb + 64 * 4096);
      LT();
    };
    const int pb = blk < 240 ? blk : blk - 16;
    const char* w2p = p.w2 + __builtin_amdgcn_readfirstlane((pb / 5) * 32 * 1280 + (pb % 5) * 256);
    asm volatile("s_waitcnt vmcnt(0)" : "+v"(dummy)::"memory");
    LT();
    issA(0); issA(1); issA(2); issA(3);
    {
      const int n = idx * 120 + lw * 30 + (lane < 30 ? lane : 29);
      const int l2 = lane < 48 ? lane : 47;
      const int r = lw * 8 + ((l2 - 32) >> 1);
      const char* a = lane < 32 ? p.w1 + (size_t)(n / 6) * 3072 + (xcd >> 1) * 768 + (n % 6) * 128
                                : w2p + r * 1280 + (l2 & 1) * 128;
      touch(dummy, a);
    }
    waitv<16>();
    bar(); step(1);
    waitv<10>(); bar(); issA(4); step(2);
    waitv<10>(); bar(); issA(5); step(3);
    waitv<10>(); bar(); issA(6); step(4);
    waitv<10>(); bar(); issA(7); step(5);
    waitv<10>(); bar(); issA(8); step(6);
    waitv<10>(); bar(); issA(9); step(7);
    waitv<10>(); bar(); issW(0, 0); step(8);
    waitv<8>();  bar(); issW(0, 1); issW(1, 0); step(9);
    waitv<9>();  bar(); issW(1, 1); step(10);
    waitv<6>();  bar(); issW(2, 0); issW(2, 1); step(11);
    unsigned vc[4];
#pragma unroll
    for (int u = 0; u < 4; ++u) {
      const int row = 8 * u + (lane >> 3), key = ((row >> 1) & 1) | (u << 1);
      vc[u] = row * 4096 + (((lane & 7) ^ key) << 4) - u * 1024;
    }
    const char* ca = (j < 2 ? p.cr : p.ci) + (size_t)(rb2 * 128 + lw * 32) * 4096 + (j & 1) * 2048;
    const unsigned vl = lane * 16;
    const char* cb = p.ws + WS_AB + (size_t)(j * 32 * 40 + cb2 * 10) * 1024 + (lw >> 1) * 40960 + (lw & 1) * 5120;
    const int rot2 = ROT2 * (idx >> 2);
    auto issCA = [&](int t) { dma4<false>(L_AR + ((t + 2) & 3) * 16384 + lw * 4096, vc[0], vc[1], vc[2], vc[3], ca + ((t + rot2) & 15) * 128); };
    auto issCB = [&](int t) {
      const unsigned m = L_R + (t & 3) * 20480 + lw * 5120;
      const char* s = cb + (size_t)((t + rot2) & 15) * 81920;
      dma4<true>(m, vl, vl, vl, vl, s);
      dma1<true>(m + 4096, vl, s + 4096);
    };
    const unsigned fao = WS_FA + (((j >> 1) * 4 + cb2) * 32 + (j & 1) * 16 + (lane & 15)) * 4;
    unsigned fa1 = 0, fa2 = 0;
    waitv<6>();  bar(); issW(3, 0); issW(3, 1); step(12);
    waitv<6>();  bar(); issW(4, 0); issW(4, 1); issW(5, 0);
    {
      const unsigned vw = (lw * 8 + (lane >> 4)) * 1280 + (lane & 15) * 16;
      dma2<false>((lw < 2 ? L_SP0 : L_SP1 - 4096) + lw * 2048, vw, vw + 4 * 1280 - 1024, w2p);
    }
    step(13);
    waitv<11>(); bar(); issW(5, 1); issW(6, 0); issW(6, 1); step(14);
    waitv<14>(); bar(); issW(7, 0); issW(7, 1); step(15);
    waitv<12>(); bar(); issW(8, 0); issW(8, 1); step(16);
    waitv<12>(); bar(); issW(9, 0); issW(9, 1); step(17);
    waitv<12>(); bar(); issCA(0); step(18);
    waitv<10>(); bar(); issCA(1); poll_req(fa1, p.ws, fao); step(19);
    waitv<9>();  bar(); poll_req(fa2, p.ws, fao); step(20);
    bar();
    asm volatile("s_waitcnt vmcnt(1)" : "+v"(fa1)::"memory");
    if (__builtin_amdgcn_ballot_w64(fa1 != tok) != 0) {
      asm volatile("s_waitcnt vmcnt(0)" : "+v"(fa2)::"memory");
      poll_spin(fa2, p.ws, fao, tok);
    }
    issCB(0); issCB(1); issCA(2); issCB(2); issCA(3); issCB(3);
    step(21);
    waitv<23>(); bar(); step(22);
    const char* da = p.ws + WS_H + (size_t)(rb2 * 2 + (j >> 1)) * 48 * 4096 + lw * 4096;
    const char* db = p.ws + WS_W2 + (size_t)(cb2 * 2 + (j & 1)) * 48 * 5120 + lw * 5120;
    const int rot3 = ROT3 * (3 * cb2 + ((3 * (idx >> 2)) >> 1));
    auto issD = [&](int d) {
      const int dr = (d + rot3) % 12;
      dma4<true>(L_AR + ((d + 2) & 3) * 16384 + lw * 4096, vl, vl, vl, vl, da + (size_t)dr * 16384);
      const unsigned m = L_R + (d & 3) * 20480 + lw * 5120;
      const char* s = db + (size_t)dr * 20480;
      dma4<true>(m, vl, vl, vl, vl, s);
      dma1<true>(m + 4096, vl, s + 4096);
    };
    u32x4 fb = {0, 0, 0, 0};
    const unsigned fbo = WS_FB + lane * 16;
    sfor<12>([&](auto CC) {
      constexpr int c = CC.value;
      waitv<18>(); bar(); issCA(c + 4); issCB(c + 4);
      if constexpr (c == 9) poll_req4(fb, p.ws, fbo);
      step(23 + c);
    });
    waitv<18>(); bar();
    poll_spin4(fb, p.ws, fbo, tok);
    issD(0); LT();
    waitv<18>(); bar(); issD(1); LT();
    waitv<18>(); bar(); issD(2); LT();
    waitv<18>(); bar(); issD(3); LT();
    const int tile = rb2 * 4 + cb2;
    unsigned fc = 0;
    const unsigned fco = WS_FC + (tile * 4 + (lane & 3)) * 4;
    sfor<8>([&](auto DD) {
      constexpr int d = DD.value;
      waitv<18>(); bar(); issD(d + 4);
      if constexpr (d == 6) poll_req(fc, p.ws, fco);
      LT();
    });
    waitv<18>(); bar();
    asm volatile("s_waitcnt vmcnt(9)" : "+v"(fc)::"memory");
    poll_spin(fc, p.ws, fco, tok);
    if (lw < 3) {
      const char* s = p.ws + WS_YP + (size_t)((tile * 4 + j) * 4 + ((j + 1 + lw) & 3)) * 10240;
      const unsigned m = lw == 0 ? L_R : lw == 1 ? L_R + 10240 : 32768u;
      dma4<true>(m, vl, vl, vl, vl, s);
      dma4<true>(m + 4096, vl, vl, vl, vl, s + 4096);
      dma2<true>(m + 8192, vl, vl, s + 8192);
    }
    LT();
    bar(); LT();
    if (lw < 3) waitv<10>(); else waitv<0>();
    bar(); LT();
    waitv<0>(); bar(); LT();
    asm volatile(".Lfnet_end:" : "+v"(dummy), "+v"(cdummy));
    DIAG_OUT();
  } else {
    const int cw = wid, wm = cw >> 1, wn = cw & 1;
    __builtin_amdgcn_s_setprio(3);
    {
      u32x4 xr[2][10];
      const unsigned xo = (rb1 * 64 + cw * 16 + (lane >> 3)) * 1280 + (lane & 7) * 16;
#define XLOAD(B, O)                                                                                                                   \
      asm volatile(                                                                                                                   \
          "global_load_dwordx4 %0, %10, %11\n\tglobal_load_dwordx4 %1, %10, %11 offset:128\n\t"                                       \
          "global_load_dwordx4 %2, %10, %11 offset:256\n\tglobal_load_dwordx4 %3, %10, %11 offset:384\n\t"                            \
          "global_load_dwordx4 %4, %10, %11 offset:512\n\tglobal_load_dwordx4 %5, %10, %11 offset:640\n\t"                            \
          "global_load_dwordx4 %6, %10, %11 offset:768\n\tglobal_load_dwordx4 %7, %10, %11 offset:896\n\t"                            \
          "global_load_dwordx4 %8, %10, %11 offset:1024\n\tglobal_load_dwordx4 %9, %10, %11 offset:1152"                              \
          : "=&v"(xr[B][0]), "=&v"(xr[B][1]), "=&v"(xr[B][2]), "=&v"(xr[B][3]), "=&v"(xr[B][4]), "=&v"(xr[B][5]), "=&v"(xr[B][6]),    \
            "=&v"(xr[B][7]), "=&v"(xr[B][8]), "=&v"(xr[B][9])                                                                         \
          : "v"(O), "s"(p.x) : "memory")
      XLOAD(0, xo);
      XLOAD(1, xo + 8 * 1280);
      sfor<2>([&](auto BB) {
        constexpr int B = BB.value;
        f32x2 v[40];
        f32x2 s = {0.f, 0.f};
        sfor<10>([&](auto KK) {
          constexpr int k = KK.value;
          asm volatile("s_waitcnt vmcnt(%2)" : "+v"(xr[B][k]), "+v"(s) : "i"(19 - B * 10 - k) : "memory");
#pragma unroll
          for (int e = 0; e < 4; ++e) { v[k * 4 + e] = bfpair(xr[B][k][e]); s += v[k * 4 + e]; }
        });
        if constexpr (B == 0) LT();
        const float mean = red8(s[0] + s[1]) * (1.0f / 640.0f);
        f32x2 s2a = {0.f, 0.f}, s2b = {0.f, 0.f};
#pragma unroll
        for (int k = 0; k < 40; k += 2) {
          v[k] -= f32x2{mean, mean}; s2a += v[k] * v[k];
          v[k + 1] -= f32x2{mean, mean}; s2b += v[k + 1] * v[k + 1];
        }
        s2a += s2b;
        const float rstd = __builtin_amdgcn_rsqf(red8(s2a[0] + s2a[1]) * (1.0f / 640.0f) + 1e-5f);
        const int row = cw * 16 + B * 8 + (lane >> 3);
        const int key = ((row >> 1) & 1) | (((row >> 3) & 3) << 1);
        const unsigned a = L_XN + row * 128 + (((lane & 7) ^ key) << 4);
        const unsigned ah = a + 65536;
        sfor<10>([&](auto KK) {
          constexpr int k = KK.value;
          u32x4 o;
#pragma unroll
          for (int e = 0; e < 4; ++e) { f32x2 t = v[k * 4 + e] * f32x2{rstd, rstd}; o[e] = pk2(t[0], t[1]); }
          if constexpr (k < 8) dsw<k * 8192>(a, o); else dsw<(k - 8) * 8192>(ah, o);
        });
      });
    }
    const int i = lane & 15, q = lane >> 4;
    AdX ax; AdA aa;
    {
      const int key2 = ((i >> 1) & 1) | ((i >> 2) << 1);
      const int r0 = wm * 32 + 8 * (i >> 2) + (i & 3);
      ax.a0 = L_XN + r0 * 128 + ((q ^ key2) << 4); ax.a1 = ax.a0 ^ 64;
      ax.a0h = ax.a0 + 65536; ax.a1h = ax.a1 + 65536;
      aa.p0 = L_R + ((wn ? 96 : 0) + 8 * (i >> 2) + (i & 3)) * 128 + ((q ^ key2) << 4); aa.p1 = aa.p0 ^ 64;
      aa.p0s = aa.p0 + 3 * 20480; aa.p1s = aa.p1 + 3 * 20480;
      aa.s0 = L_R + ((wn ? 80 : 64) + i) * 128 + ((q ^ (i >> 1)) << 4); aa.s1 = aa.s0 ^ 64;
      asm volatile("" : "+v"(ax.a0), "+v"(ax.a1), "+v"(ax.a0h), "+v"(ax.a1h));
      asm volatile("" : "+v"(aa.p0), "+v"(aa.p1), "+v"(aa.p0s), "+v"(aa.p1s), "+v"(aa.s0), "+v"(aa.s1));
    }
    const int gq = lane >> 4, qq = (lane >> 2) & 3, pp = lane & 3;
    AdB ab;
    {
      const int g = ((qq >> 1) & 1) | ((gq & 1) << 1);
#pragma unroll
      for (int n = 0; n < 4; ++n) {
        ab.e[n] = (8 * gq + qq) * 384 + (((6 * wn + n) ^ g) << 5) + pp * 8;
        ab.r[n] = ab.e[n] + L_R; ab.r2[n] = ab.r[n] + 65536;
        asm volatile("" : "+v"(ab.e[n]), "+v"(ab.r[n]), "+v"(ab.r2[n]));
      }
    }
    waitl<0>();
    LT();
    bar();
    LT();
    FrB xb, yb;
    {
      f32x4 acc[2][5];
      FrA x, y;
      rdA_first(x, ax, aa, mkseq<7>{});
      waitl<0>();
      sfor<10>([&](auto TT) {
        constexpr int T = TT.value;
        halfA<T, 1, T == 0>(acc, x, y, ax, aa, mkseq<10>{});
        waitl<0>(); bar();
        if constexpr (T < 9) halfA<T + 1, 0, false>(acc, y, x, ax, aa, mkseq<10>{});
        else halfA_last(acc, y, xb, ax, ab, mkseq<10>{});
        waitl<0>();
      });
      LT();
      mfma_settle();
      unsigned ao = WS_AB + (((cb1 >> 2) * 64 + rb1 * 2 + wm) * 40 + (cb1 & 3) * 10 + wn * 5) * 1024 + lane * 16;
      sfor<5>([&](auto NN_) {
        constexpr int n = NN_.value;
        u32x4 o = {pk2(acc[0][n][0], acc[0][n][1]), pk2(acc[0][n][2], acc[0][n][3]), pk2(acc[1][n][0], acc[1][n][1]), pk2(acc[1][n][2], acc[1][n][3])};
        if constexpr (n == 4) { ao += 4096; asm volatile("" : "+v"(ao)); }
        gst16<(n & 3) * 1024>(p.ws, ao, o);
      });
      LT();
    }
    {
      f32x4 acc[2][6];
      sfor<10>([&](auto UU) {
        constexpr int U = UU.value;
        halfB<U, 1, true, U == 0>(acc, xb, yb, ax, ab, mkseq<12>{});
        waitl<0>();
        if constexpr (U == 4) waitv<0>();
        bar();
        if constexpr (U == 4) {
          if (cw == 0 && lane == 0) gst4(p.ws, WS_FA + (cb1 * 32 + rb1) * 4, tok);
          const int B5 = (blk % 5) * 4 + cw, r5 = B5 % 5, dg = B5 / 5;
          const bool single = r5 == 2;
          const unsigned ta = L_SP0 + (4 * gq + qq) * 256 + cw * 64 + (single ? pp * 8 : pp * 16);
          const unsigned tb2 = ta + (single ? 32 : 8);
          bf16x4 t[2][2];
          dstr<0>(t[0][0], ta); dstr<L_SP1 - L_SP0>(t[0][1], ta); dstr<0>(t[1][0], tb2); dstr<L_SP1 - L_SP0>(t[1][1], tb2);
          asm volatile("s_waitcnt lgkmcnt(0)" : "+v"(t[0][0]), "+v"(t[0][1]), "+v"(t[1][0]), "+v"(t[1][1]));
#pragma unroll
          for (int b = 0; b < 2; ++b) {
            const int qg = 2 * dg + (r5 > 2 ? 1 : (single ? b : 0));
            const int n = single ? 4 : ((r5 == 1 || r5 == 4) ? 2 + b : b);
            const unsigned wo = (blk < 240 ? WS_W2 + ((qg * 48 + blk / 5) * 5 + n) * 1024 : WS_DUM + (cw * 2 + b) * 1024) + lane * 16;
            gst16<0>(p.ws, wo, __builtin_bit_cast(u32x4, cat(t[b][0], t[b][1])));
          }
        }
        halfB<U + 1, 0, (U < 9), false>(acc, yb, xb, ax, ab, mkseq<12>{});
        if constexpr (U < 9) waitl<0>();
      });
      LT();
      mfma_settle();
      const unsigned ho = WS_H + ((rb1 * 48 + cb1 * 6 + wn * 3) * 4 + wm * 2) * 1024 + lane * 16;
      sfor<2>([&](auto MM) {
        constexpr int m = MM.value;
        f32x2 v[12];
#pragma unroll
        for (int n = 0; n < 6; ++n) { v[2 * n] = f32x2{acc[m][n][0], acc[m][n][1]}; v[2 * n + 1] = f32x2{acc[m][n][2], acc[m][n][3]}; }
        silu_pairs<12>(v);
#pragma unroll
        for (int pr = 0; pr < 3; ++pr) {
          u32x4 o = {pk2(v[4 * pr][0], v[4 * pr][1]), pk2(v[4 * pr + 1][0], v[4 * pr + 1][1]), pk2(v[4 * pr + 2][0], v[4 * pr + 2][1]), pk2(v[4 * pr + 3][0], v[4 * pr + 3][1])};
          gst16<m * 1024>(p.ws, ho + pr * 4096, o);
        }
      });
      LT();
    }
    if (wn == 0) stage23<3>(p, cw, lane, blk, wid, tok, rb2, cb2, j DIAG_PASS);
    else stage23<2>(p, cw, lane, blk, wid, tok, rb2, cb2, j DIAG_PASS);
    DIAG_OUT();
  }
}

extern "C" void kernel_launch(void* const* d_in, const int* in_sizes, int n_in, void* d_out, int out_size, void* d_ws, size_t ws_size,
                              hipStream_t stream) {
  Params p;
  p.x = (const char*)d_in[0]; p.cr = (const char*)d_in[1]; p.ci = (const char*)d_in[2];
  p.hr = (const char*)d_in[3]; p.hi = (const char*)d_in[4]; p.w1 = (const char*)d_in[6]; p.w2 = (const char*)d_in[7];
  p.out = (char*)d_out; p.ws = (char*)d_ws;
  fnet_fused<<<256, 512, 0, stream>>>(p);
}
```

```cpp
#include <hip/hip_runtime.h>
#include <stdint.h>
#include <utility>
#define XSWAP 0
#define HT_DELAY 8

#define DI __device__ __forceinline__
typedef short bf16x8 __attribute__((ext_vector_type(8)));
typedef short bf16x4 __attribute__((ext_vector_type(4)));
typedef float f32x4 __attribute__((ext_vector_type(4)));
typedef float f32x2 __attribute__((ext_vector_type(2)));
typedef unsigned u32x4 __attribute__((ext_vector_type(4)));
typedef unsigned u32x2 __attribute__((ext_vector_type(2)));
typedef unsigned long long u64;
template <int... I> using iseq = std::integer_sequence<int, I...>;
template <int N> using mkseq = std::make_integer_sequence<int, N>;
template <int N> struct IC { static constexpr int value = N; };
template <class F, int... I> DI void sfor_impl(F&& f, iseq<I...>) { (f(IC<I>{}), ...); }
template <int N, class F> DI void sfor(F&& f) { sfor_impl(f, mkseq<N>{}); }

struct Params {
  const char* x; const char* cr; const char* ci; const char* hr; const char* hi;
  const char* w1; const char* w2; char* out; char* ws;
};

constexpr unsigned WS_AB = 0;
constexpr unsigned WS_H  = WS_AB + 5242880;
constexpr unsigned WS_W2 = WS_H + 6291456;
constexpr unsigned WS_YP = WS_W2 + 1966080;
constexpr unsigned WS_DUM = WS_YP + 10485760;
constexpr unsigned WS_FA = WS_DUM + 8192;
constexpr unsigned WS_FB = WS_FA + 1024;
constexpr unsigned WS_FC = WS_FB + 1024;
constexpr unsigned WS_HB = WS_FC + 1024;
constexpr unsigned WS_END = WS_HB + 512;

constexpr unsigned L_XN = 0;
constexpr unsigned L_R = 81920;
constexpr unsigned L_AR = 0;
constexpr unsigned L_OWN = 65536;
constexpr unsigned L_SP0 = L_R + 36864, L_SP1 = L_R + 77824;
constexpr unsigned L_SIZE = 163840;
constexpr unsigned w1slot(int u, int ks) {
  constexpr unsigned H0 = L_R + 40960, H1 = L_R + 53248, H2 = L_R + 65536, H3 = L_R, H4 = L_R + 12288, H5 = L_R + 24576, E0 = 0, E1 = 12288;
  switch (u) {
    case 0: case 3: case 7: return ks ? H1 : H0;
    case 1: case 4: case 8: return ks ? H3 : H2;
    case 2: case 6: return ks ? H5 : H4;
    default: return ks ? E1 : E0;
  }
}

#define LTL() do {} while (0)
#define LTXL(n) do {} while (0)
#define EP0 1
#define EPN 6
#define ROT2 0
#define ROT3 0
#define SPIN_MAX 2000000

template <int OFF> DI void dsr(bf16x8& d, unsigned a) { static_assert(OFF >= 0 && OFF < 65536, ""); asm volatile("ds_read_b128 %0, %1 offset:%2" : "=v"(d) : "v"(a), "i"(OFF)); }
template <int OFF> DI void dsr4(u32x4& d, unsigned a) { static_assert(OFF >= 0 && OFF < 65536, ""); asm volatile("ds_read_b128 %0, %1 offset:%2" : "=v"(d) : "v"(a), "i"(OFF)); }
template <int OFF> DI void dstr(bf16x4& d, unsigned a) { static_assert(OFF >= 0 && OFF < 65536, ""); asm volatile("ds_read_b64_tr_b16 %0, %1 offset:%2" : "=v"(d) : "v"(a), "i"(OFF)); }
template <int OFF> DI void dsw(unsigned a, const u32x4& d) { static_assert(OFF >= 0 && OFF < 65536, ""); asm volatile("ds_write_b128 %0, %1 offset:%2" ::"v"(a), "v"(d), "i"(OFF) : "memory"); }
template <bool Z> DI void mfma(f32x4& c, const bf16x8& a, const bf16x8& b) {
  if constexpr (Z) asm volatile("v_mfma_f32_16x16x32_bf16 %0, %1, %2, 0" : "=&v"(c) : "v"(a), "v"(b));
  else asm volatile("v_mfma_f32_16x16x32_bf16 %0, %1, %2, %0" : "+v"(c) : "v"(a), "v"(b));
}
template <int N> DI void waitl() { static_assert(N >= 0 && N < 16, ""); __builtin_amdgcn_s_waitcnt(0xC07F | (N << 8)); }
template <int N> DI void waitv() { static_assert(N >= 0 && N < 64, ""); asm volatile("s_waitcnt vmcnt(%0)" ::"i"(N) : "memory"); }
DI void bar() { asm volatile("s_barrier" ::: "memory"); }
DI void mfma_settle() { asm volatile("s_nop 7\n\ts_nop 7" ::: "memory"); }
DI void sched_fence() { __builtin_amdgcn_sched_barrier(0); }

#define DMA_HEAD "s_mov_b32 m0, %0\n\ts_nop 0\n\t"
#define DMA4(POL)                                                                                         \
  asm volatile(DMA_HEAD "global_load_lds_dwordx4 %1, %5 " POL "\n\t"                                      \
               "global_load_lds_dwordx4 %2, %5 offset:1024 " POL "\n\t"                                   \
               "global_load_lds_dwordx4 %3, %5 offset:2048 " POL "\n\t"                                   \
               "global_load_lds_dwordx4 %4, %5 offset:3072 " POL ::"s"(m), "v"(v0), "v"(v1), "v"(v2), "v"(v3), "s"(s) : "memory")
template <bool SC1> DI void dma4(unsigned m, unsigned v0, unsigned v1, unsigned v2, unsigned v3, const char* s) {
  if constexpr (SC1) DMA4("sc1"); else DMA4("");
}
#define DMA3(POL)                                                                                         \
  asm volatile(DMA_HEAD "global_load_lds_dwordx4 %1, %4 " POL "\n\t"                                      \
               "global_load_lds_dwordx4 %2, %4 offset:1024 " POL "\n\t"                                   \
               "global_load_lds_dwordx4 %3, %4 offset:2048 " POL ::"s"(m), "v"(v0), "v"(v1), "v"(v2), "s"(s) : "memory")
template <bool SC1> DI void dma3(unsigned m, unsigned v0, unsigned v1, unsigned v2, const char* s) {
  if constexpr (SC1) DMA3("sc1"); else DMA3("");
}
#define DMA2(POL)                                                                                         \
  asm volatile(DMA_HEAD "global_load_lds_dwordx4 %1, %3 " POL "\n\t"                                      \
               "global_load_lds_dwordx4 %2, %3 offset:1024 " POL ::"s"(m), "v"(v0), "v"(v1), "s"(s) : "memory")
template <bool SC1> DI void dma2(unsigned m, unsigned v0, unsigned v1, const char* s) {
  if constexpr (SC1) DMA2("sc1"); else DMA2("");
}
#define DMA1(POL) asm volatile(DMA_HEAD "global_load_lds_dwordx4 %1, %2 " POL ::"s"(m), "v"(v0), "s"(s) : "memory")
template <bool SC1> DI void dma1(unsigned m, unsigned v0, const char* s) {
  if constexpr (SC1) DMA1("sc1"); else DMA1("");
}

DI void setm0(unsigned m) { asm volatile("s_mov_b32 m0, %0\n\ts_nop 0" ::"s"(m) : "memory"); }
template <int OFF> DI void dmai(unsigned v, const char* s) { asm volatile("global_load_lds_dwordx4 %0, %1 offset:%2" ::"v"(v), "s"(s), "i"(OFF) : "memory"); }
DI f32x2 pkfma_v(const f32x2& a, const f32x2& b, const f32x2& c) { f32x2 r; asm volatile("v_pk_fma_f32 %0, %1, %2, %3" : "=v"(r) : "v"(a), "v"(b), "v"(c)); return r; }
DI unsigned pk2_v(const f32x2& t) { unsigned r; asm volatile("v_cvt_pk_bf16_f32 %0, %1, %2" : "=v"(r) : "v"(t[0]), "v"(t[1])); return r; }
#define LN_DOTS                                                                                              \
  "v_dot2_f32_bf16 %0, %8, %12, %14\n\tv_dot2_f32_bf16 %1, %8, %13, %14\n\t"                                \
  "v_dot2_f32_bf16 %2, %9, %12, %14\n\tv_dot2_f32_bf16 %3, %9, %13, %14\n\t"                                \
  "v_dot2_f32_bf16 %4, %10, %12, %14\n\tv_dot2_f32_bf16 %5, %10, %13, %14\n\t"                              \
  "v_dot2_f32_bf16 %6, %11, %12, %14\n\tv_dot2_f32_bf16 %7, %11, %13, %14"
#define LN_OUT "=&v"(t[0]), "=&v"(t[1]), "=&v"(t[2]), "=&v"(t[3]), "=&v"(t[4]), "=&v"(t[5]), "=&v"(t[6]), "=&v"(t[7])
DI void ln_acc(const float (&t)[8], f32x2* o, f32x2& s1, f32x2& s2) {
#pragma unroll
  for (int e = 0; e < 4; ++e) {
    o[e] = f32x2{t[2 * e], t[2 * e + 1]};
    s1 += o[e];
    s2 = __builtin_elementwise_fma(o[e], o[e], s2);
  }
}
DI void ln_chunk(const u32x4& x, unsigned slo, unsigned shi, float nk, f32x2* o, f32x2& s1, f32x2& s2) {
  float t[8];
  asm volatile(LN_DOTS : LN_OUT : "v"(x[0]), "v"(x[1]), "v"(x[2]), "v"(x[3]), "s"(slo), "s"(shi), "v"(nk));
  ln_acc(t, o, s1, s2);
}
template <int N, bool LD, int OFF>
DI void ln_chunk_w(const u32x4& x, unsigned slo, unsigned shi, float nk, f32x2* o, f32x2& s1, f32x2& s2, u32x4& y, unsigned off, const char* p) {
  float t[8];
  if constexpr (LD)
    asm volatile("s_waitcnt vmcnt(%18)\n\t"
                 "v_dot2_f32_bf16 %0, %9, %13, %15\n\tv_dot2_f32_bf16 %1, %9, %14, %15\n\t"
                 "v_dot2_f32_bf16 %2, %10, %13, %15\n\tv_dot2_f32_bf16 %3, %10, %14, %15\n\t"
                 "v_dot2_f32_bf16 %4, %11, %13, %15\n\tv_dot2_f32_bf16 %5, %11, %14, %15\n\t"
                 "v_dot2_f32_bf16 %6, %12, %13, %15\n\tv_dot2_f32_bf16 %7, %12, %14, %15\n\t"
                 "global_load_dwordx4 %8, %16, %17 offset:%19"
                 : LN_OUT, "=&v"(y)
                 : "v"(x[0]), "v"(x[1]), "v"(x[2]), "v"(x[3]), "s"(slo), "s"(shi), "v"(nk), "v"(off), "s"(p), "i"(N), "i"(OFF) : "memory");
  else
    asm volatile("s_waitcnt vmcnt(%15)\n\t" LN_DOTS : LN_OUT
                 : "v"(x[0]), "v"(x[1]), "v"(x[2]), "v"(x[3]), "s"(slo), "s"(shi), "v"(nk), "i"(N) : "memory");
  ln_acc(t, o, s1, s2);
}

template <int OFF> DI void gst16(const char* base, unsigned voff, const u32x4& d) {
  static_assert(OFF >= 0 && OFF < 4096, "");
  asm volatile("global_store_dwordx4 %0, %1, %2 offset:%3 sc1\n\ts_nop 1" ::"v"(voff), "v"(d), "s"(base), "i"(OFF) : "memory");
}
DI void gst4(const char* base, unsigned voff, unsigned d) {
  asm volatile("global_store_dword %0, %1, %2 sc1" ::"v"(voff), "v"(d), "s"(base) : "memory");
}
DI void gst8(const char* base, unsigned voff, const u32x2& d) {
  asm volatile("global_store_dwordx2 %0, %1, %2 sc1\n\ts_nop 1" ::"v"(voff), "v"(d), "s"(base) : "memory");
}
DI unsigned rfl(unsigned v) { return __builtin_amdgcn_readfirstlane(v); }
typedef __bf16 bf16p __attribute__((ext_vector_type(2)));
DI unsigned pk2(float lo, float hi) { return __builtin_bit_cast(unsigned, __builtin_convertvector(f32x2{lo, hi}, bf16p)); }
DI float bflo(unsigned v) { return __uint_as_float(v << 16); }
DI float bfhi(unsigned v) { return __uint_as_float(v & 0xffff0000u); }
DI f32x2 bfpair(unsigned v) { return f32x2{bflo(v), bfhi(v)}; }
template <int CTRL> DI float dppadd(float v) {
  return v + __int_as_float(__builtin_amdgcn_update_dpp(0, __float_as_int(v), CTRL, 0xf, 0xf, true));
}
DI float red8(float v) { v = dppadd<0xB1>(v); v = dppadd<0x4E>(v); return dppadd<0x141>(v); }
template <int N> DI void silu_pairs(f32x2 (&v)[N]) {
  f32x2 t[N];
#pragma unroll
  for (int k = 0; k < N; ++k) t[k] = v[k] * f32x2{-1.44269504088896341f, -1.44269504088896341f};
  sched_fence();
#pragma unroll
  for (int k = 0; k < N; ++k) { t[k][0] = __builtin_amdgcn_exp2f(t[k][0]); t[k][1] = __builtin_amdgcn_exp2f(t[k][1]); }
  sched_fence();
#pragma unroll
  for (int k = 0; k < N; ++k) t[k] += f32x2{1.0f, 1.0f};
  sched_fence();
#pragma unroll
  for (int k = 0; k < N; ++k) { t[k][0] = __builtin_amdgcn_rcpf(t[k][0]); t[k][1] = __builtin_amdgcn_rcpf(t[k][1]); }
  sched_fence();
#pragma unroll
  for (int k = 0; k < N; ++k) v[k] *= t[k];
  sched_fence();
}

DI void touch(unsigned& d, const char* a) { asm volatile("global_load_dword %0, %1, off" : "+v"(d) : "v"(a) : "memory"); }
DI void touchs(unsigned& d, unsigned voff, const char* base) { asm volatile("global_load_dword %0, %1, %2" : "+v"(d) : "v"(voff), "s"(base) : "memory"); }
DI void touch2_if(unsigned& d, int n, int ep, unsigned voff, const char* b0, const char* b1) {
  asm volatile("s_cmp_lg_u32 %1, %2\n\ts_cbranch_scc1 .Lskip_touch%=\n\t"
               "global_load_dword %0, %3, %4\n\tglobal_load_dword %0, %3, %5\n"
               ".Lskip_touch%=:" : "+v"(d) : "s"(n), "s"(ep), "v"(voff), "s"(b0), "s"(b1) : "memory", "scc");
}

DI void poll_req(unsigned& fv, const char* base, unsigned voff) {
  asm volatile("global_load_dword %0, %1, %2 sc1" : "+v"(fv) : "v"(voff), "s"(base) : "memory");
}
DI void poll_req4(u32x4& fv, const char* base, unsigned voff) {
  asm volatile("global_load_dwordx4 %0, %1, %2 sc1" : "+v"(fv) : "v"(voff), "s"(base) : "memory");
}
DI void poll_spin(unsigned& fv, const char* base, unsigned voff, unsigned tok) {
  asm volatile("" : "+v"(fv));
  for (int n = 0; __builtin_amdgcn_ballot_w64(fv != tok) != 0 && n < SPIN_MAX; ++n)
    asm volatile("global_load_dword %0, %1, %2 sc1\n\ts_waitcnt vmcnt(0)" : "=v"(fv) : "v"(voff), "s"(base) : "memory");
}
DI void poll_spin4(u32x4& fv, const char* base, unsigned voff, unsigned tok) {
  asm volatile("" : "+v"(fv));
  for (int n = 0; __builtin_amdgcn_ballot_w64(fv[0] != tok || fv[1] != tok || fv[2] != tok || fv[3] != tok) != 0 && n < SPIN_MAX; ++n)
    asm volatile("global_load_dwordx4 %0, %1, %2 sc1\n\ts_waitcnt vmcnt(0)" : "=v"(fv) : "v"(voff), "s"(base) : "memory");
}

#define RTW 3
#define LT() do {} while (0)
#define RT() do {} while (0)
#define LTX(n) do {} while (0)
#define RTX(n) do {} while (0)
#define DIAG_DECL do {} while (0)
#define DIAG_OUT() do {} while (0)
#define DIAG_ARGS
#define DIAG_PASS

struct FrB { bf16x8 a[2]; bf16x4 w[6][2]; };
struct AdX { unsigned a0, a1, a0h, a1h; };
struct AdB { unsigned e[4], r[4], r2[4]; };
template <int I, int T, int KS> DI void rdX(bf16x8& d, const AdX& ad) {
  dsr<(T & 7) * 8192 + I * 512>(d, T < 8 ? (KS ? ad.a1 : ad.a0) : (KS ? ad.a1h : ad.a0h));
}
template <int I, int U, int KS> DI void rdB(FrB& f, const AdX& ax, const AdB& ad) {
  if constexpr (I < 2) rdX<I, U, KS>(f.a[I], ax);
  else {
    constexpr int n = (I - 2) >> 1, h = (I - 2) & 1;
    constexpr unsigned base = w1slot(U, KS), in = h * 1536 + (n >> 2) * 128;
    if constexpr (base < L_R) dstr<base + in>(f.w[n][h], ad.e[n & 3]);
    else if constexpr (base - L_R < 65536) dstr<base - L_R + in>(f.w[n][h], ad.r[n & 3]);
    else dstr<base - L_R - 65536 + in>(f.w[n][h], ad.r2[n & 3]);
  }
}

struct FrA { bf16x8 a[2], b[5]; };
struct AdA { unsigned p0, p1, p0s, p1s, s0, s1; };
template <int I, int T, int KS> DI void rdA(FrA& f, const AdX& ax, const AdA& ad) {
  if constexpr (I < 2) rdX<I, T, KS>(f.a[I], ax);
  else {
    constexpr int n = I - 2, s = T & 3;
    if constexpr (n == 4) dsr<s * 20480>(f.b[4], KS ? ad.s1 : ad.s0);
    else {
      constexpr int in = (n >> 1) * 4096 + (n & 1) * 512;
      if constexpr (s < 3) dsr<s * 20480 + in>(f.b[n], KS ? ad.p1 : ad.p0);
      else dsr<in>(f.b[n], KS ? ad.p1s : ad.p0s);
    }
  }
}
template <int G, int T, int KS> DI void gapA(FrA& y, const AdX& ax, const AdA& ad) {
  if constexpr (G < 4) { rdA<2 * G, T, KS>(y, ax, ad); if constexpr (2 * G + 1 < 7) rdA<2 * G + 1, T, KS>(y, ax, ad); }
}
template <int T, int KS, bool Z, int... G> DI void halfA(f32x4 (&acc)[2][5], FrA& x, FrA& y, const AdX& ax, const AdA& ad, iseq<G...>) {
  ((mfma<Z>(acc[G / 5][G % 5], x.a[G / 5], x.b[G % 5]), gapA<G, T, KS>(y, ax, ad)), ...);
}
template <int G> DI void gapAB(FrB& xb, const AdX& ax, const AdB& ab) {
  if constexpr (G < 7) { rdB<2 * G, 0, 0>(xb, ax, ab); rdB<2 * G + 1, 0, 0>(xb, ax, ab); }
}
template <int... G> DI void halfA_last(f32x4 (&acc)[2][5], FrA& y, FrB& xb, const AdX& ax, const AdB& ab, iseq<G...>) {
  ((mfma<false>(acc[G / 5][G % 5], y.a[G / 5], y.b[G % 5]), gapAB<G>(xb, ax, ab)), ...);
}
template <int... I> DI void rdA_first(FrA& x, const AdX& ax, const AdA& ad, iseq<I...>) { (rdA<I, 0, 0>(x, ax, ad), ...); }

template <int G, int U, int KS, bool RD> DI void gapB(FrB& y, const AdX& ax, const AdB& ad) {
  if constexpr (RD && G < 7) { rdB<2 * G, U, KS>(y, ax, ad); rdB<2 * G + 1, U, KS>(y, ax, ad); }
}
DI bf16x8 cat(const bf16x4& l, const bf16x4& h) { return __builtin_shufflevector(l, h, 0, 1, 2, 3, 4, 5, 6, 7); }
template <int U, int KS, bool RD, bool Z, int... G> DI void halfB(f32x4 (&acc)[2][6], FrB& x, FrB& y, const AdX& ax, const AdB& ad, iseq<G...>) {
  ((mfma<Z>(acc[G / 6][G % 6], cat(x.w[G % 6][0], x.w[G % 6][1]), x.a[G / 6]), gapB<G, U, KS, RD>(y, ax, ad)), ...);
}

struct FrC { bf16x8 a[4], b[5]; };
struct AdC { unsigned a0, a1, b0, b1; };
template <int I, int S, int KS> DI void rdC(FrC& f, const AdC& ad) {
  if constexpr (I >= 1 && I <= 5) {
    constexpr int n = I - 1, off = S * 20480 + KS * 10240 + n * 1024;
    if constexpr (S < 3) dsr<off>(f.b[n], ad.b0); else dsr<off - 3 * 20480>(f.b[n], ad.b1);
  } else {
    constexpr int m = (I == 0) ? 0 : I - 5, off = ((S + 2) & 3) * 16384 + (m >> 1) * 4096 + (m & 1) * 512;
    dsr<off>(f.a[m], KS ? ad.a1 : ad.a0);
  }
}
template <int G, int S> DI void gapC0(FrC& y, const AdC& ad) {
  if constexpr (G < 9) rdC<G, S, 1>(y, ad);
  if constexpr (G == 4) waitl<7>();
  if constexpr (G == 9) waitl<10>();
  if constexpr (G == 14) waitl<9>();
}
template <int G, int S> DI void gapC1(FrC& x, const AdC& ad) {
  if constexpr (G < 9) rdC<G, (S + 1) & 3, 0>(x, ad);
  if constexpr (G == 19) waitl<3>();
}
template <int S, int... G> DI void halfC0(f32x4 (&acc)[4][5], FrC& x, FrC& y, const AdC& ad, iseq<G...>) {
  ((mfma<false>(acc[G / 5][G % 5], x.b[G % 5], x.a[G / 5]), gapC0<G, S>(y, ad)), ...);
}
template <int S, int... G> DI void halfC1(f32x4 (&acc)[4][5], FrC& x, FrC& y, const AdC& ad, iseq<G...>) {
  ((mfma<false>(acc[G / 5][G % 5], y.b[G % 5], y.a[G / 5]), gapC1<G, S>(x, ad)), ...);
}
template <int... I> DI void rdC_first(FrC& x, const AdC& ad, iseq<I...>) { (rdC<I, 0, 0>(x, ad), ...); }

template <int NN> struct FrD { bf16x8 h[2][2], w[2][NN]; };
struct AdD { unsigned a, b, bs; };
template <int NN> constexpr int nfrag(int k) { return NN == 3 ? (k == 2 ? 4 : k) : 2 + k; }
template <int NN, int I, int D, int HF> DI void rdD(FrD<NN>& f, const AdD& ad) {
  constexpr int kk = I / (NN + 2), r = I % (NN + 2), ks = HF * 2 + kk;
  if constexpr (r < 2) dsr<((D + 2) & 3) * 16384 + ks * 4096 + r * 1024>(f.h[kk][r], ad.a);
  else if constexpr ((D & 3) < 3) dsr<(D & 3) * 20480 + ks * 5120 + nfrag<NN>(r - 2) * 1024>(f.w[kk][r - 2], ad.b);
  else dsr<ks * 5120 + nfrag<NN>(r - 2) * 1024>(f.w[kk][r - 2], ad.bs);
}
template <int NN, int G, int D, int HF> DI void gapD(FrD<NN>& y, const AdD& ad) {
  if constexpr (2 * G < 2 * (NN + 2)) rdD<NN, 2 * G, D, HF>(y, ad);
  if constexpr (2 * G + 1 < 2 * (NN + 2)) rdD<NN, 2 * G + 1, D, HF>(y, ad);
}
template <int NN, int G> DI void mfD(f32x4 (&acc)[2][NN], FrD<NN>& x) {
  constexpr int kk = G / (2 * NN), m = (G % (2 * NN)) / NN, n = G % NN;
  mfma<false>(acc[m][n], x.w[kk][n], x.h[kk][m]);
}
template <int NN, int D, int HF, int... G> DI void halfD(f32x4 (&acc)[2][NN], FrD<NN>& x, FrD<NN>& y, const AdD& ad, iseq<G...>) {
  ((mfD<NN, G>(acc, x), gapD<NN, G, D, HF>(y, ad)), ...);
}
template <int NN, int G> DI void gapCD(FrD<NN>& x, const AdD& ad) { if constexpr (G < 2 * (NN + 2)) rdD<NN, G, 0, 0>(x, ad); }
template <int NN, int... G> DI void halfC_last(f32x4 (&acc)[4][5], FrC& y, FrD<NN>& xd, const AdD& ad, iseq<G...>) {
  ((mfma<false>(acc[G / 5][G % 5], y.b[G % 5], y.a[G / 5]), gapCD<NN, G>(xd, ad)), ...);
}
template <int NN, int G> DI void gapY(u32x4 (&yp)[4][NN], const unsigned (&ya)[4]) {
  if constexpr (G < 2 * NN) {
    constexpr int i0 = 2 * G, i1 = 2 * G + 1;
    dsr4<nfrag<NN>(i0 % NN) * 1024>(yp[i0 / NN][i0 % NN], ya[i0 / NN]);
    dsr4<nfrag<NN>(i1 % NN) * 1024>(yp[i1 / NN][i1 % NN], ya[i1 / NN]);
  }
}
template <int NN, int... G> DI void halfD_last(f32x4 (&acc)[2][NN], FrD<NN>& y, u32x4 (&yp)[4][NN], const unsigned (&ya)[4], iseq<G...>) {
  ((mfD<NN, G>(acc, y), gapY<NN, G>(yp, ya)), ...);
}

template <int NN>
DI void stage23(const Params& p, int cw, int lane, int blk, int wid, unsigned tok, int rb2, int cb2, int j DIAG_ARGS) {
  const int wm3 = cw >> 1;
  const int tile = rb2 * 4 + cb2;
  const int i = lane & 15, q = lane >> 4, par = j & 1;
  f32x4 acc2[4][5];
  FrC xc, yc;
  {
    AdC ad;
    const int wm = cw >> 1, wn = NN == 3 ? 0 : 1;
    const int key2 = ((i >> 1) & 1) | ((i >> 2) << 1);
    const int r0 = wm * 64 + 8 * (i >> 2) + (i & 3);
    ad.a0 = L_AR + r0 * 128 + ((q ^ key2) << 4); ad.a1 = ad.a0 ^ 64;
    ad.b0 = L_R + wn * 5120 + lane * 16; ad.b1 = ad.b0 + 3 * 20480;
    asm volatile("" : "+v"(ad.a0), "+v"(ad.a1), "+v"(ad.b0), "+v"(ad.b1));
#pragma unroll
    for (int m = 0; m < 4; ++m)
#pragma unroll
      for (int n = 0; n < 5; ++n) { acc2[m][n] = f32x4{0, 0, 0, 0}; asm volatile("" : "+v"(acc2[m][n])); }
    bar();
    LT();
    rdC_first(xc, ad, mkseq<9>{});
    waitl<0>();
#pragma unroll 1
    for (int g = 0; g < 4; ++g) {
      halfC0<0>(acc2, xc, yc, ad, mkseq<20>{}); waitl<0>();
      if (g == 1) waitv<0>();
      bar();
      if (g == 1 && cw == 0 && lane == 0) gst4(p.ws, WS_FB + blk * 4, tok);
      halfC1<0>(acc2, xc, yc, ad, mkseq<20>{});
      halfC0<1>(acc2, xc, yc, ad, mkseq<20>{}); waitl<0>(); bar();
      halfC1<1>(acc2, xc, yc, ad, mkseq<20>{});
      halfC0<2>(acc2, xc, yc, ad, mkseq<20>{}); waitl<0>(); bar();
      halfC1<2>(acc2, xc, yc, ad, mkseq<20>{});
      halfC0<3>(acc2, xc, yc, ad, mkseq<20>{}); waitl<0>(); bar();
      if (g < 3) halfC1<3>(acc2, xc, yc, ad, mkseq<20>{});
    }
  }
  AdD ad;
  ad.a = L_AR + wm3 * 2048 + lane * 16;
  ad.b = L_R + lane * 16;
  ad.bs = ad.b + 3 * 20480;
  asm volatile("" : "+v"(ad.a), "+v"(ad.b), "+v"(ad.bs));
  FrD<NN> x, y;
  halfC_last<NN>(acc2, yc, x, ad, mkseq<20>{});
  LT();
  mfma_settle();
  if (cw == j) {
    const unsigned a = L_OWN + lane * 16;
    sfor<10>([&](auto K) {
      constexpr int pr = K.value / 5, n = K.value % 5;
      u32x4 o = {pk2(acc2[2 * pr][n][0], acc2[2 * pr][n][1]), pk2(acc2[2 * pr][n][2], acc2[2 * pr][n][3]),
                 pk2(acc2[2 * pr + 1][n][0], acc2[2 * pr + 1][n][1]), pk2(acc2[2 * pr + 1][n][2], acc2[2 * pr + 1][n][3])};
      dsw<K.value * 1024>(a, o);
    });
  } else {
    unsigned yo = WS_YP + ((tile * 4 + cw) * 4 + j) * 10240 + lane * 16;
    sfor<10>([&](auto K) {
      constexpr int pr = K.value / 5, n = K.value % 5;
      u32x4 o = {pk2(acc2[2 * pr][n][0], acc2[2 * pr][n][1]), pk2(acc2[2 * pr][n][2], acc2[2 * pr][n][3]),
                 pk2(acc2[2 * pr + 1][n][0], acc2[2 * pr + 1][n][1]), pk2(acc2[2 * pr + 1][n][2], acc2[2 * pr + 1][n][3])};
      if constexpr (K.value == 4 || K.value == 8) { yo += 4096; asm volatile("" : "+v"(yo)); }
      gst16<(K.value & 3) * 1024>(p.ws, yo, o);
    });
  }
  f32x4 acc[2][NN];
#pragma unroll
  for (int m = 0; m < 2; ++m)
#pragma unroll
    for (int n = 0; n < NN; ++n) { acc[m][n] = f32x4{0, 0, 0, 0}; asm volatile("" : "+v"(acc[m][n])); }
  waitl<0>();
  LT();
  const unsigned trow = rb2 * 128 + 64 * (j >> 1) + 32 * wm3 + 8 * (i >> 2) + (i & 3);
  const unsigned ecol = cb2 * 160 + 80 * par;
  const unsigned xop = trow * 1280 + (ecol + (NN == 3 ? (par ? 16 : 0) : (par ? 48 : 32))) * 2 + q * 16;
  const unsigned xos = trow * 1280 + (ecol + (par ? 0 : 64)) * 2 + q * 8;
  unsigned ya[4];
#pragma unroll
  for (int jj = 0; jj < 4; ++jj) {
    const int k = (jj - j - 1) & 3;
    ya[jj] = (k == 0 ? L_R : k == 1 ? L_R + 10240 : k == 2 ? 32768u : L_OWN) + wm3 * 5120 + lane * 16;
    asm volatile("" : "+v"(ya[jj]));
  }
  u32x4 xp[2];
  u32x2 xs[2];
  u32x4 yp[4][NN];
  sfor<12>([&](auto DD) {
    constexpr int D = DD.value;
    halfD<NN, D, 1>(acc, x, y, ad, mkseq<4 * NN>{});
    waitl<0>();
    if constexpr (D == 3) waitv<0>();
    bar();
    if constexpr (D == 3) { if (cw == 0 && lane == 0) gst4(p.ws, WS_FC + (tile * 4 + j) * 4, tok); }
    if constexpr (D == 8) {
      asm volatile("global_load_dwordx4 %0, %2, %3\n\tglobal_load_dwordx4 %1, %4, %3" : "=&v"(xp[0]), "=&v"(xp[1]) : "v"(xop), "s"(p.x), "v"(xop + 4 * 1280) : "memory");
      if constexpr (NN == 3)
        asm volatile("global_load_dwordx2 %0, %2, %3\n\tglobal_load_dwordx2 %1, %4, %3" : "=&v"(xs[0]), "=&v"(xs[1]) : "v"(xos), "s"(p.x), "v"(xos + 4 * 1280) : "memory");
    }
    if constexpr (D < 11) { halfD<NN, D + 1, 0>(acc, y, x, ad, mkseq<4 * NN>{}); waitl<0>(); }
    else halfD_last<NN>(acc, y, yp, ya, mkseq<4 * NN>{});
  });
  waitl<0>();
  LT();
  if constexpr (NN == 3) asm volatile("s_waitcnt vmcnt(0)" : "+v"(xp[0]), "+v"(xp[1]), "+v"(xs[0]), "+v"(xs[1])::"memory");
  else asm volatile("s_waitcnt vmcnt(0)" : "+v"(xp[0]), "+v"(xp[1])::"memory");
#pragma unroll
  for (int jj = 0; jj < 4; ++jj)
#pragma unroll
    for (int n = 0; n < NN; ++n) asm volatile("" : "+v"(yp[jj][n]));
  f32x2 ys[2][NN][2];
#pragma unroll
  for (int m = 0; m < 2; ++m)
#pragma unroll
    for (int n = 0; n < NN; ++n)
#pragma unroll
      for (int h = 0; h < 2; ++h)
        ys[m][n][h] = (bfpair(yp[0][n][m * 2 + h]) + bfpair(yp[1][n][m * 2 + h])) - (bfpair(yp[2][n][m * 2 + h]) + bfpair(yp[3][n][m * 2 + h]));
  sched_fence();
  mfma_settle();
  f32x2 o[2 * NN * 2];
#pragma unroll
  for (int m = 0; m < 2; ++m)
#pragma unroll
    for (int n = 0; n < NN; ++n) { o[(m * NN + n) * 2] = f32x2{acc[m][n][0], acc[m][n][1]}; o[(m * NN + n) * 2 + 1] = f32x2{acc[m][n][2], acc[m][n][3]}; }
  silu_pairs<2 * NN * 2>(o);
#pragma unroll
  for (int m = 0; m < 2; ++m) {
    u32x4 r;
#pragma unroll
    for (int n = 0; n < 2; ++n)
#pragma unroll
      for (int h = 0; h < 2; ++h) {
        f32x2 t = bfpair(xp[m][n * 2 + h]) + ys[m][n][h] * o[(m * NN + n) * 2 + h];
        r[n * 2 + h] = pk2(t[0], t[1]);
      }
    asm volatile("global_store_dwordx4 %0, %1, %2 sc1\n\ts_nop 1" ::"v"(xop + m * 4 * 1280), "v"(r), "s"(p.out) : "memory");
    if constexpr (NN == 3) {
      u32x2 r2;
#pragma unroll
      for (int h = 0; h < 2; ++h) {
        f32x2 t = bfpair(xs[m][h]) + ys[m][2][h] * o[(m * NN + 2) * 2 + h];
        r2[h] = pk2(t[0], t[1]);
      }
      asm volatile("global_store_dwordx2 %0, %1, %2 sc1\n\ts_nop 1" ::"v"(xos + m * 4 * 1280), "v"(r2), "s"(p.out) : "memory");
    }
  }
  LT();
}

__global__ __launch_bounds__(512, 2) void fnet_fused(Params p) {
  __shared__ __attribute__((aligned(16))) char lds[L_SIZE];
  const int tid = threadIdx.x, lane = tid & 63;
  const int wid = __builtin_amdgcn_readfirstlane(tid >> 6);
  const int blk = blockIdx.x, xcd = blk & 7, idx = blk >> 3;
  const int rb1 = (xcd & 1) * 16 + (idx >> 1), cb1 = (xcd >> 1) * 2 + (idx & 1);
  const int j = xcd & 3, rb2 = (xcd >> 2) * 8 + (idx >> 2), cb2 = idx & 3;
  asm volatile("" ::"v"((unsigned)(size_t)lds));
  DIAG_DECL;
  RT(); LT();
  const int lw = wid & 3;

  int leader = rfl((blk | wid) == 0 ? 1 : 0);
  asm volatile("" : "+s"(leader));
  const unsigned ho = WS_HB + ((leader ? lane : wid) & 3) * 128;
  unsigned tl = 0;
  unsigned hbk = 0;
  auto hb_post = [&]() { if (leader) { ++hbk; gst8(p.ws, WS_HB + (lane & 3) * 128, u32x2{hbk, tl}); } };
  const int pb = blk < 240 ? blk : blk - 16;

  if (wid >= 4) {
    unsigned dummy = 0;
    u32x2 hb0;
    asm volatile("global_load_dwordx2 %0, %1, %2 sc1" : "=&v"(hb0) : "v"(ho), "s"(p.ws) : "memory");
    __builtin_amdgcn_s_sleep(HT_DELAY);
    {
      const char* hb = ((xcd >> 1) < 2 ? p.hr : p.hi) + (size_t)((xcd >> 1) & 1) * 320 * 1280;
      touchs(dummy, (idx * 100 + lw * 25 + (lane < 24 ? lane : 24)) * 128, hb);
    }
    unsigned va[5];
#pragma unroll
    for (int u = 0; u < 5; ++u) {
      const int P = lw * 5 + u, r = lane >> 3;
      const int ka = (P >> 2) == 2 ? (P & 1) << 2 : (P & 3) << 1, km = (P >> 2) == 2 ? 3 : 1;
      va[u] = (P * 8 + r) * 1280 + (((lane & 7) ^ (ka | ((r >> 1) & km))) << 4) - (u < 4 ? u * 1024 : 0);
      asm volatile("" : "+v"(va[u]));
    }
    const char* ha = (cb1 < 4 ? p.hr : p.hi) + (size_t)(cb1 & 3) * 160 * 1280;
    unsigned vb[3];
#pragma unroll
    for (int u = 0; u < 3; ++u) {
      const int G = u * 64 + lane, row = lw * 8 + G / 24, c = G % 24;
      const int g = ((row >> 1) & 1) | (((row >> 3) & 1) << 1);
      vb[u] = row * 3072 + ((c ^ (g << 1)) << 4) - u * 1024;
      asm volatile("" : "+v"(vb[u]));
    }
    const char* w2p = p.w2 + rfl((pb / 5) * 32 * 1280 + (pb % 5) * 256);
    unsigned wt1, wt2;
    {
      const unsigned n = idx * 120 + lw * 30 + (lane < 29 ? lane : 29), n6 = (n * 43691u) >> 18;
      wt1 = n6 * 3072 + (n - n6 * 6) * 128 + (xcd >> 1) * 768;
      const int l2 = lane < 15 ? lane : 15;
      wt2 = (lw * 8 + (l2 >> 1)) * 1280 + (l2 & 1) * 128;
      asm volatile("" : "+v"(wt1), "+v"(wt2));
    }
    asm volatile("s_waitcnt vmcnt(0)" : "+v"(dummy), "+v"(hb0)::"memory");
    const unsigned h00 = rfl(hb0[0]), h01 = rfl(hb0[1]);
    bar();
    auto issA = [&](int t) {
      const unsigned m = L_R + (t & 3) * 20480 + lw * 5120;
      dma4<false>(m, va[0], va[1], va[2], va[3], ha + t * 128);
      dma1<false>(m + 4096, va[4], ha + t * 128);
    };
    const char* wb = p.w1 + cb1 * 384;
    auto issW = [&](int u, int ks) { dma3<false>(w1slot(u, ks) + lw * 3072, vb[0], vb[1], vb[2], wb + (size_t)(u * 64 + ks * 32) * 3072); };
    const int ep = EP0 + (((idx >> 1) * EPN) >> 2);
    const char* tb = (j < 2 ? p.cr : p.ci) + (size_t)((xcd >> 2) * 1024 + (idx & 1) * 512 + lw * 128) * 4096 + (j & 1) * 2048 + (((idx >> 1) + ROT2 * ((idx & 1) * 4 + lw)) & 15) * 128;
    const unsigned tvo = lane * 4096;
    auto step = [&](int n) {
      touch2_if(dummy, n, ep, tvo, tb, tb + 64 * 4096);
      LTL();
    };
    LTL();
    issA(0); issA(1); issA(2); issA(3);
    touchs(dummy, wt1, p.w1);
    touchs(dummy, wt2, w2p);
    waitv<17>();
    LTL();
    bar(); step(1);
    waitv<12>(); bar(); issA(4); step(2);
    waitv<12>(); bar(); issA(5); step(3);
    waitv<10>(); bar(); issA(6); step(4);
    waitv<10>(); bar(); issA(7); step(5);
    waitv<10>(); bar(); issA(8); step(6);
    u32x2 hb1;
    waitv<10>(); bar(); issA(9);
    asm volatile("global_load_dwordx2 %0, %1, %2 sc1" : "=v"(hb1) : "v"(ho), "s"(p.ws) : "memory");
    step(7);
    waitv<10>(); bar(); issW(0, 0); step(8);
    waitv<8>();  bar(); issW(0, 1); issW(1, 0); step(9);
    asm volatile("s_waitcnt vmcnt(9)" : "+v"(hb1)::"memory");
    for (int n = 0; rfl(hb1[0]) == h00 && rfl(hb1[1]) == h01 && n < SPIN_MAX; ++n)
      asm volatile("global_load_dwordx2 %0, %1, %2 sc1\n\ts_waitcnt vmcnt(0)" : "=v"(hb1) : "v"(ho), "s"(p.ws) : "memory");
    const unsigned tok = rfl(hb1[1]);
    bar(); issW(1, 1); step(10);
    waitv<6>();  bar(); issW(2, 0); issW(2, 1); step(11);
    unsigned vc[4];
#pragma unroll
    for (int u = 0; u < 4; ++u) {
      const int row = 8 * u + (lane >> 3), key = ((row >> 1) & 1) | (u << 1);
      vc[u] = row * 4096 + (((lane & 7) ^ key) << 4) - u * 1024;
    }
    const char* ca = (j < 2 ? p.cr : p.ci) + (size_t)(rb2 * 128 + lw * 32) * 4096 + (j & 1) * 2048;
    const unsigned vl = lane * 16;
    const char* cb = p.ws + WS_AB + (size_t)(j * 32 * 40 + cb2 * 10) * 1024 + (lw >> 1) * 40960 + (lw & 1) * 5120;
    const int rot2 = ROT2 * (idx >> 2);
    auto issCA = [&](int t) { dma4<false>(L_AR + ((t + 2) & 3) * 16384 + lw * 4096, vc[0], vc[1], vc[2], vc[3], ca + ((t + rot2) & 15) * 128); };
    auto issCB = [&](int t) {
      const unsigned m = L_R + (t & 3) * 20480 + lw * 5120;
      const char* s = cb + (size_t)((t + rot2) & 15) * 81920;
      dma4<true>(m, vl, vl, vl, vl, s);
      dma1<true>(m + 4096, vl, s + 4096);
    };
    const unsigned fao = WS_FA + (((j >> 1) * 4 + cb2) * 32 + (j & 1) * 16 + (lane & 15)) * 4;
    unsigned fa1 = 0, fa2 = 0;
    waitv<6>();  bar(); issW(3, 0); issW(3, 1); step(12);
    waitv<6>();  bar(); issW(4, 0); issW(4, 1); issW(5, 0);
    {
      const unsigned vw = (lw * 8 + (lane >> 4)) * 1280 + (lane & 15) * 16;
      dma2<false>((lw < 2 ? L_SP0 : L_SP1 - 4096) + lw * 2048, vw, vw + 4 * 1280 - 1024, w2p);
    }
    step(13);
    waitv<11>(); bar(); issW(5, 1); issW(6, 0); issW(6, 1); step(14);
    waitv<14>(); bar(); issW(7, 0); issW(7, 1); step(15);
    waitv<12>(); bar(); issW(8, 0); issW(8, 1); step(16);
    waitv<12>(); bar(); issW(9, 0); issW(9, 1); step(17);
    waitv<12>(); bar(); issCA(0); step(18);
    waitv<10>(); bar(); issCA(1); poll_req(fa1, p.ws, fao); step(19);
    waitv<9>();  bar(); poll_req(fa2, p.ws, fao); step(20);
    bar();
    asm volatile("s_waitcnt vmcnt(1)" : "+v"(fa1)::"memory");
    if (__builtin_amdgcn_ballot_w64(fa1 != tok) != 0) {
      asm volatile("s_waitcnt vmcnt(0)" : "+v"(fa2)::"memory");
      poll_spin(fa2, p.ws, fao, tok);
    }
    issCB(0); issCB(1); issCA(2); issCB(2); issCA(3); issCB(3);
    step(21);
    waitv<23>(); bar(); step(22);
    const char* da = p.ws + WS_H + (size_t)(rb2 * 2 + (j >> 1)) * 48 * 4096 + lw * 4096;
    const char* db = p.ws + WS_W2 + (size_t)(cb2 * 2 + (j & 1)) * 48 * 5120 + lw * 5120;
    const int rot3 = ROT3 * (3 * cb2 + ((3 * (idx >> 2)) >> 1));
    auto issD = [&](int d) {
      const int dr = (d + rot3) % 12;
      dma4<true>(L_AR + ((d + 2) & 3) * 16384 + lw * 4096, vl, vl, vl, vl, da + (size_t)dr * 16384);
      const unsigned m = L_R + (d & 3) * 20480 + lw * 5120;
      const char* s = db + (size_t)dr * 20480;
      dma4<true>(m, vl, vl, vl, vl, s);
      dma1<true>(m + 4096, vl, s + 4096);
    };
    u32x4 fb = {0, 0, 0, 0};
    const unsigned fbo = WS_FB + lane * 16;
    sfor<12>([&](auto CC) {
      constexpr int c = CC.value;
      waitv<18>(); bar();
      issCA(c + 4); issCB(c + 4);
      if constexpr (c == 9) poll_req4(fb, p.ws, fbo);
      step(23 + c);
    });
    waitv<18>(); bar();
    poll_spin4(fb, p.ws, fbo, tok);
    issD(0); LTL();
    waitv<18>(); bar(); issD(1); LTL();
    waitv<18>(); bar(); issD(2); LTL();
    waitv<18>(); bar(); issD(3); LTL();
    const int tile = rb2 * 4 + cb2;
    unsigned fc = 0;
    const unsigned fco = WS_FC + (tile * 4 + (lane & 3)) * 4;
    sfor<8>([&](auto DD) {
      constexpr int d = DD.value;
      waitv<18>(); bar();
      issD(d + 4);
      if constexpr (d == 6) poll_req(fc, p.ws, fco);
      LTL();
    });
    waitv<18>(); bar();
    asm volatile("s_waitcnt vmcnt(9)" : "+v"(fc)::"memory");
    poll_spin(fc, p.ws, fco, tok);
    if (lw < 3) {
      const char* s = p.ws + WS_YP + (size_t)((tile * 4 + j) * 4 + ((j + 1 + lw) & 3)) * 10240;
      const unsigned m = lw == 0 ? L_R : lw == 1 ? L_R + 10240 : 32768u;
      dma4<true>(m, vl, vl, vl, vl, s);
      dma4<true>(m + 4096, vl, vl, vl, vl, s + 4096);
      dma2<true>(m + 8192, vl, vl, s + 8192);
    }
    LTL();
    bar(); LTL();
    if (lw < 3) waitv<10>(); else waitv<0>();
    bar(); LTL();
    waitv<0>(); bar(); LTL();
    asm volatile("" : "+v"(dummy));
    DIAG_OUT();
  } else {
    const int cw = wid, wm = cw >> 1, wn = cw & 1;
    u32x2 hb0;
    u32x4 st0, st1, st2;
    asm volatile("s_cmp_lg_u32 %3, 0\n\ts_cbranch_scc1 .Lskip_hb%=\n\tglobal_load_dwordx2 %0, %1, %2 sc1\n.Lskip_hb%=:"
                 : "=&v"(hb0) : "v"(ho), "s"(p.ws), "s"(cw) : "memory", "scc");
    asm volatile("s_cmp_eq_u32 %5, 0\n\ts_cbranch_scc1 .Lskip_st%=\n\tglobal_load_dwordx4 %0, %3, %4 sc1\n\t"
                 "global_load_dwordx4 %1, %3, %4 offset:1024 sc1\n\tglobal_load_dwordx4 %2, %3, %4 offset:2048 sc1\n.Lskip_st%=:"
                 : "=&v"(st0), "=&v"(st1), "=&v"(st2) : "v"(WS_FA + lane * 16), "s"(p.ws), "s"(leader) : "memory", "scc");
    u32x4 xr[2][10];
    const int xsw = XSWAP * ((xcd >> 1) & 1);
    const unsigned xo = (rb1 * 64 + cw * 16 + xsw * 8 + (lane >> 3)) * 1280 + (lane & 7) * 16;
    asm volatile(
        "global_load_dwordx4 %0, %10, %11\n\tglobal_load_dwordx4 %1, %10, %11 offset:128\n\t"
        "global_load_dwordx4 %2, %10, %11 offset:256\n\tglobal_load_dwordx4 %3, %10, %11 offset:384\n\t"
        "global_load_dwordx4 %4, %10, %11 offset:512\n\tglobal_load_dwordx4 %5, %10, %11 offset:640\n\t"
        "global_load_dwordx4 %6, %10, %11 offset:768\n\tglobal_load_dwordx4 %7, %10, %11 offset:896\n\t"
        "global_load_dwordx4 %8, %10, %11 offset:1024\n\tglobal_load_dwordx4 %9, %10, %11 offset:1152"
        : "=&v"(xr[0][0]), "=&v"(xr[0][1]), "=&v"(xr[0][2]), "=&v"(xr[0][3]), "=&v"(xr[0][4]), "=&v"(xr[0][5]), "=&v"(xr[0][6]),
          "=&v"(xr[0][7]), "=&v"(xr[0][8]), "=&v"(xr[0][9])
        : "v"(xo), "s"(p.x) : "memory");
    unsigned xo1 = xo + (8 - 16 * xsw) * 1280;
    asm volatile("" : "+v"(xo1));
    LTX(60);
    const int i = lane & 15, q = lane >> 4;
    AdX ax; AdA aa; AdB ab;
    const int gq = lane >> 4, qq = (lane >> 2) & 3, pp = lane & 3;
    {
      const int key2 = ((i >> 1) & 1) | ((i >> 2) << 1);
      const int r0 = wm * 32 + 8 * (i >> 2) + (i & 3);
      ax.a0 = L_XN + r0 * 128 + ((q ^ key2) << 4); ax.a1 = ax.a0 ^ 64;
      ax.a0h = ax.a0 + 65536; ax.a1h = ax.a1 + 65536;
      aa.p0 = L_R + ((wn ? 96 : 0) + 8 * (i >> 2) + (i & 3)) * 128 + ((q ^ key2) << 4); aa.p1 = aa.p0 ^ 64;
      aa.p0s = aa.p0 + 3 * 20480; aa.p1s = aa.p1 + 3 * 20480;
      aa.s0 = L_R + ((wn ? 80 : 64) + i) * 128 + ((q ^ (i >> 1)) << 4); aa.s1 = aa.s0 ^ 64;
      asm volatile("" : "+v"(ax.a0), "+v"(ax.a1), "+v"(ax.a0h), "+v"(ax.a1h));
      asm volatile("" : "+v"(aa.p0), "+v"(aa.p1), "+v"(aa.p0s), "+v"(aa.p1s), "+v"(aa.s0), "+v"(aa.s1));
      const int g = ((qq >> 1) & 1) | ((gq & 1) << 1);
#pragma unroll
      for (int n = 0; n < 4; ++n) {
        ab.e[n] = (8 * gq + qq) * 384 + (((6 * wn + n) ^ g) << 5) + pp * 8;
        ab.r[n] = ab.e[n] + L_R; ab.r2[n] = ab.r[n] + 65536;
        asm volatile("" : "+v"(ab.e[n]), "+v"(ab.r[n]), "+v"(ab.r2[n]));
      }
    }
    unsigned xa[2], xah[2];
#pragma unroll
    for (int B = 0; B < 2; ++B) {
      const int row = cw * 16 + (B ^ xsw) * 8 + (lane >> 3);
      const int key = ((row >> 1) & 1) | (((row >> 3) & 3) << 1);
      xa[B] = L_XN + row * 128 + (((lane & 7) ^ key) << 4);
      xah[B] = xa[B] + 65536;
      asm volatile("" : "+v"(xa[B]), "+v"(xah[B]));
    }
    unsigned h00 = 0, h01 = 0;
    {
      unsigned sel_lo = 0x00003F80u, sel_hi = 0x3F800000u;
      asm volatile("" : "+s"(sel_lo), "+s"(sel_hi));
      f32x2 d0[40], d1[40], s1, s2, rs, cs;
      float nk;
      auto shift = [&](unsigned v) {
        int t = __builtin_amdgcn_update_dpp(0, (int)(v << 16), 0x00, 0xf, 0xf, true);
        t = __builtin_amdgcn_update_dpp(t, t, 0x141, 0xf, 0xa, false);
        return -__int_as_float(t);
      };
      auto finish = [&]() {
        const float md = red8(s1[0] + s1[1]) * (1.0f / 640.0f);
        const float var = fmaxf(red8(s2[0] + s2[1]) * (1.0f / 640.0f) - md * md, 0.0f);
        const float rstd = __builtin_amdgcn_rsqf(var + 1e-5f), c = -md * rstd;
        rs = f32x2{rstd, rstd}; cs = f32x2{c, c};
        s1 = s2 = f32x2{0.f, 0.f};
      };
      auto norm = [&](auto KK, auto BB, const f32x2* d) {
        constexpr int K = KK.value, B = BB.value;
        u32x4 o;
#pragma unroll
        for (int e = 0; e < 4; ++e) { const f32x2 t = __builtin_elementwise_fma(d[K * 4 + e], rs, cs); o[e] = pk2(t[0], t[1]); }
        if constexpr (K < 8) dsw<K * 8192>(xa[B], o); else dsw<(K - 8) * 8192>(xah[B], o);
      };
      s1 = s2 = f32x2{0.f, 0.f};
      sfor<10>([&](auto KK) {
        constexpr int k = KK.value;
        if constexpr (k == 0) {
          asm volatile("s_waitcnt vmcnt(9)" : "+v"(xr[0][0]), "+v"(hb0), "+v"(st0), "+v"(st1), "+v"(st2)::"memory");
          LTX(61);
          asm volatile("global_load_dwordx4 %0, %1, %2" : "=&v"(xr[1][0]) : "v"(xo1), "s"(p.x) : "memory");
          h00 = rfl(hb0[0]); h01 = rfl(hb0[1]);
          if (leader) {
            unsigned c = h01 + 1;
#pragma unroll 1
            for (int n = 0; n < 1024; ++n, ++c) {
              bool hit = hb0[1] == c;
#pragma unroll
              for (int e = 0; e < 4; ++e) hit |= st0[e] == c || st1[e] == c || st2[e] == c;
              if (__builtin_amdgcn_ballot_w64(hit) == 0) break;
            }
            tl = c; hbk = h00;
            hb_post();
          }
          nk = shift(xr[0][0][0]);
          ln_chunk(xr[0][0], sel_lo, sel_hi, nk, &d0[0], s1, s2);
        } else ln_chunk_w<9, true, k * 128>(xr[0][k], sel_lo, sel_hi, nk, &d0[k * 4], s1, s2, xr[1][k], xo1, p.x);
      });
      bar();
      LT();
      finish();
      LTX(56);
      sfor<10>([&](auto KK) {
        constexpr int k = KK.value;
        if constexpr (k == 0) {
          asm volatile("s_waitcnt vmcnt(9)" : "+v"(xr[1][0])::"memory");
          nk = shift(xr[1][0][0]);
          ln_chunk(xr[1][0], sel_lo, sel_hi, nk, &d1[0], s1, s2);
        } else ln_chunk_w<9 - k, false, 0>(xr[1][k], sel_lo, sel_hi, nk, &d1[k * 4], s1, s2, xr[1][k], 0, p.x);
        norm(KK, IC<0>{}, d0);
      });
      LTX(62);
      finish();
      LTX(58);
      sfor<10>([&](auto KK) { norm(KK, IC<1>{}, d1); });
      LTX(59);
    }
    LT();
    waitl<0>();
    bar();
    LT();
    hb_post();
    FrB xb, yb;
    u32x2 hb1;
    unsigned tok = tl;
    {
      f32x4 acc[2][5];
      FrA x, y;
      rdA_first(x, ax, aa, mkseq<7>{});
      waitl<0>();
      sfor<10>([&](auto TT) {
        constexpr int T = TT.value;
        halfA<T, 1, T == 0>(acc, x, y, ax, aa, mkseq<10>{});
        waitl<0>();
        bar();
        if constexpr (T == 3 || T == 6) hb_post();
        if constexpr (T < 9) halfA<T + 1, 0, false>(acc, y, x, ax, aa, mkseq<10>{});
        else halfA_last(acc, y, xb, ax, ab, mkseq<10>{});
        waitl<0>();
      });
      LT();
      asm volatile("s_cmp_lg_u32 %3, 0\n\ts_cbranch_scc1 .Lskip_h1%=\n\tglobal_load_dwordx2 %0, %1, %2 sc1\n.Lskip_h1%=:"
                   : "=&v"(hb1) : "v"(ho), "s"(p.ws), "s"(cw) : "memory", "scc");
      mfma_settle();
      unsigned ao = WS_AB + (((cb1 >> 2) * 64 + rb1 * 2 + wm) * 40 + (cb1 & 3) * 10 + wn * 5) * 1024 + lane * 16;
      sfor<5>([&](auto NN_) {
        constexpr int n = NN_.value;
        u32x4 o = {pk2(acc[0][n][0], acc[0][n][1]), pk2(acc[0][n][2], acc[0][n][3]), pk2(acc[1][n][0], acc[1][n][1]), pk2(acc[1][n][2], acc[1][n][3])};
        if constexpr (n == 4) { ao += 4096; asm volatile("" : "+v"(ao)); }
        gst16<(n & 3) * 1024>(p.ws, ao, o);
      });
      LT();
    }
    {
      f32x4 acc[2][6];
      sfor<10>([&](auto UU) {
        constexpr int U = UU.value;
        halfB<U, 1, true, U == 0>(acc, xb, yb, ax, ab, mkseq<12>{});
        waitl<0>();
        if constexpr (U == 4) asm volatile("s_waitcnt vmcnt(0)" : "+v"(hb1)::"memory");
        bar();
        if constexpr (U == 4) {
          if (cw == 0) {
            if (!leader) {
              for (int n = 0; rfl(hb1[0]) == h00 && rfl(hb1[1]) == h01 && n < SPIN_MAX; ++n)
                asm volatile("global_load_dwordx2 %0, %1, %2 sc1\n\ts_waitcnt vmcnt(0)" : "=v"(hb1) : "v"(ho), "s"(p.ws) : "memory");
              tok = rfl(hb1[1]);
            }
            if (lane == 0) gst4(p.ws, WS_FA + (cb1 * 32 + rb1) * 4, tok);
          }
          const int B5 = (blk % 5) * 4 + cw, r5 = B5 % 5, dg = B5 / 5;
          const bool single = r5 == 2;
          const unsigned ta = L_SP0 + (4 * gq + qq) * 256 + cw * 64 + (single ? pp * 8 : pp * 16);
          const unsigned tb2 = ta + (single ? 32 : 8);
          bf16x4 t[2][2];
          dstr<0>(t[0][0], ta); dstr<L_SP1 - L_SP0>(t[0][1], ta); dstr<0>(t[1][0], tb2); dstr<L_SP1 - L_SP0>(t[1][1], tb2);
          asm volatile("s_waitcnt lgkmcnt(0)" : "+v"(t[0][0]), "+v"(t[0][1]), "+v"(t[1][0]), "+v"(t[1][1]));
#pragma unroll
          for (int b = 0; b < 2; ++b) {
            const int qg = 2 * dg + (r5 > 2 ? 1 : (single ? b : 0));
            const int n = single ? 4 : ((r5 == 1 || r5 == 4) ? 2 + b : b);
            const unsigned wo = (blk < 240 ? WS_W2 + ((qg * 48 + blk / 5) * 5 + n) * 1024 : WS_DUM + (cw * 2 + b) * 1024) + lane * 16;
            gst16<0>(p.ws, wo, __builtin_bit_cast(u32x4, cat(t[b][0], t[b][1])));
          }
        }
        halfB<U + 1, 0, (U < 9), false>(acc, yb, xb, ax, ab, mkseq<12>{});
        if constexpr (U < 9) waitl<0>();
      });
      LT();
      u32x4 fchk = {~tl, ~tl, ~tl, ~tl};
      asm volatile("s_cmp_eq_u32 %3, 0\n\ts_cbranch_scc1 .Lskip_fa%=\n\tglobal_load_dwordx4 %0, %1, %2 sc1\n.Lskip_fa%=:"
                   : "+v"(fchk) : "v"(WS_FA + lane * 16), "s"(p.ws), "s"(leader) : "memory", "scc");
      mfma_settle();
      const unsigned hoff = WS_H + ((rb1 * 48 + cb1 * 6 + wn * 3) * 4 + wm * 2) * 1024 + lane * 16;
      sfor<2>([&](auto MM) {
        constexpr int m = MM.value;
        f32x2 v[12];
#pragma unroll
        for (int n = 0; n < 6; ++n) { v[2 * n] = f32x2{acc[m][n][0], acc[m][n][1]}; v[2 * n + 1] = f32x2{acc[m][n][2], acc[m][n][3]}; }
        silu_pairs<12>(v);
#pragma unroll
        for (int pr = 0; pr < 3; ++pr) {
          u32x4 o = {pk2(v[4 * pr][0], v[4 * pr][1]), pk2(v[4 * pr + 1][0], v[4 * pr + 1][1]), pk2(v[4 * pr + 2][0], v[4 * pr + 2][1]), pk2(v[4 * pr + 3][0], v[4 * pr + 3][1])};
          gst16<m * 1024>(p.ws, hoff + pr * 4096, o);
        }
      });
      asm volatile("s_waitcnt vmcnt(6)" : "+v"(fchk)::"memory");
      if (leader) {
        for (int n = 0; __builtin_amdgcn_ballot_w64(fchk[0] != tl || fchk[1] != tl || fchk[2] != tl || fchk[3] != tl) != 0 && n < SPIN_MAX; ++n) {
          hb_post();
          asm volatile("global_load_dwordx4 %0, %1, %2 sc1\n\ts_waitcnt vmcnt(0)" : "=v"(fchk) : "v"(WS_FA + lane * 16), "s"(p.ws) : "memory");
        }
      }
      LT();
    }
    if (wn == 0) stage23<3>(p, cw, lane, blk, wid, tok, rb2, cb2, j DIAG_PASS);
    else stage23<2>(p, cw, lane, blk, wid, tok, rb2, cb2, j DIAG_PASS);
    RTX(63);
    DIAG_OUT();
  }
}

extern "C" void kernel_launch(void* const* d_in, const int* in_sizes, int n_in, void* d_out, int out_size, void* d_ws, size_t ws_size,
                              hipStream_t stream) {
  Params p;
  p.x = (const char*)d_in[0]; p.cr = (const char*)d_in[1]; p.ci = (const char*)d_in[2];
  p.hr = (const char*)d_in[3]; p.hi = (const char*)d_in[4]; p.w1 = (const char*)d_in[6]; p.w2 = (const char*)d_in[7];
  p.out = (char*)d_out; p.ws = (char*)d_ws;
  fnet_fused<<<256, 512, 0, stream>>>(p);
}
```

```cpp
#include <hip/hip_runtime.h>
#include <stdint.h>
#include <utility>

#define DI __device__ __forceinline__
typedef short bf16x8 __attribute__((ext_vector_type(8)));
typedef short bf16x4 __attribute__((ext_vector_type(4)));
typedef float f32x4 __attribute__((ext_vector_type(4)));
typedef float f32x2 __attribute__((ext_vector_type(2)));
typedef unsigned u32x4 __attribute__((ext_vector_type(4)));
typedef unsigned u32x2 __attribute__((ext_vector_type(2)));
typedef unsigned long long u64;
template <int... I> using iseq = std::integer_sequence<int, I...>;
template <int N> using mkseq = std::make_integer_sequence<int, N>;
template <int N> struct IC { static constexpr int value = N; };
template <class F, int... I> DI void sfor_impl(F&& f, iseq<I...>) { (f(IC<I>{}), ...); }
template <int N, class F> DI void sfor(F&& f) { sfor_impl(f, mkseq<N>{}); }

struct Params {
  const char* x; const char* cr; const char* ci; const char* hr; const char* hi;
  const char* w1; const char* w2; char* out; char* ws;
};

constexpr unsigned AB_SLICE = 1 ? 2097152 : 1310720, H_GRP = 1 ? 2097152 : 1572864;
constexpr unsigned WS_AB = 0;
constexpr unsigned WS_H  = WS_AB + 4 * AB_SLICE;
constexpr unsigned WS_W2 = WS_H + 4 * H_GRP;
constexpr unsigned WS_YP = WS_W2 + (1 ? 2097152 : 1966080);
constexpr unsigned WS_DUM = WS_YP + 10485760;
constexpr unsigned WS_FA = WS_DUM + 8192;
constexpr unsigned WS_FB = WS_FA + 1024;
constexpr unsigned WS_FC = WS_FB + 1024;
constexpr unsigned WS_HB = WS_FC + 1024;
constexpr unsigned WS_END = WS_HB + 512;

constexpr unsigned L_XN = 0;
constexpr unsigned L_R = 81920;
constexpr unsigned L_AR = 0;
constexpr unsigned L_OWN = 65536;
constexpr unsigned L_SP0 = L_R + 36864, L_SP1 = L_R + 77824;
constexpr unsigned L_SIZE = 163840;
constexpr int BSH = 0;
constexpr unsigned w1slot(int u, int ks) {
  constexpr unsigned H0 = L_R + 40960, H1 = L_R + 53248, H2 = L_R + 65536, H3 = L_R, H4 = L_R + 12288, H5 = L_R + 24576, E0 = 0, E1 = 12288;
  switch (u) {
    case 0: case 3: case 7: return ks ? H1 : H0;
    case 1: case 4: case 8: return ks ? H3 : H2;
    case 2: case 6: return ks ? H5 : H4;
    default: return ks ? E1 : E0;
  }
}

#define SPIN_MAX 2000000

template <int OFF> DI void dsr(bf16x8& d, unsigned a) { static_assert(OFF >= 0 && OFF < 65536, ""); asm volatile("ds_read_b128 %0, %1 offset:%2" : "=v"(d) : "v"(a), "i"(OFF)); }
template <int OFF> DI void dsr4(u32x4& d, unsigned a) { static_assert(OFF >= 0 && OFF < 65536, ""); asm volatile("ds_read_b128 %0, %1 offset:%2" : "=v"(d) : "v"(a), "i"(OFF)); }
template <int OFF> DI void dstr(bf16x4& d, unsigned a) { static_assert(OFF >= 0 && OFF < 65536, ""); asm volatile("ds_read_b64_tr_b16 %0, %1 offset:%2" : "=v"(d) : "v"(a), "i"(OFF)); }
template <int OFF> DI void dsw(unsigned a, const u32x4& d) { static_assert(OFF >= 0 && OFF < 65536, ""); asm volatile("ds_write_b128 %0, %1 offset:%2" ::"v"(a), "v"(d), "i"(OFF) : "memory"); }
template <bool Z> DI void mfma(f32x4& c, const bf16x8& a, const bf16x8& b) {
  if constexpr (Z) asm volatile("v_mfma_f32_16x16x32_bf16 %0, %1, %2, 0" : "=&v"(c) : "v"(a), "v"(b));
  else asm volatile("v_mfma_f32_16x16x32_bf16 %0, %1, %2, %0" : "+v"(c) : "v"(a), "v"(b));
}
template <int N> DI void waitl() { static_assert(N >= 0 && N < 16, ""); __builtin_amdgcn_s_waitcnt(0xC07F | (N << 8)); }
template <int N> DI void waitv() { static_assert(N >= 0 && N < 64, ""); asm volatile("s_waitcnt vmcnt(%0)" ::"i"(N) : "memory"); }
DI void bar() { asm volatile("s_barrier" ::: "memory"); }
DI void mfma_settle() { asm volatile("s_nop 7\n\ts_nop 7" ::: "memory"); }
DI void sched_fence() { __builtin_amdgcn_sched_barrier(0); }

#define DMA_HEAD "s_mov_b32 m0, %0\n\ts_nop 0\n\t"
#define DMA4(POL)                                                                                         \
  asm volatile(DMA_HEAD "global_load_lds_dwordx4 %1, %5 " POL "\n\t"                                      \
               "global_load_lds_dwordx4 %2, %5 offset:1024 " POL "\n\t"                                   \
               "global_load_lds_dwordx4 %3, %5 offset:2048 " POL "\n\t"                                   \
               "global_load_lds_dwordx4 %4, %5 offset:3072 " POL ::"s"(m), "v"(v0), "v"(v1), "v"(v2), "v"(v3), "s"(s) : "memory")
template <bool SC1> DI void dma4(unsigned m, unsigned v0, unsigned v1, unsigned v2, unsigned v3, const char* s) {
  if constexpr (SC1) DMA4("sc1"); else DMA4("");
}
#define DMA3(POL)                                                                                         \
  asm volatile(DMA_HEAD "global_load_lds_dwordx4 %1, %4 " POL "\n\t"                                      \
               "global_load_lds_dwordx4 %2, %4 offset:1024 " POL "\n\t"                                   \
               "global_load_lds_dwordx4 %3, %4 offset:2048 " POL ::"s"(m), "v"(v0), "v"(v1), "v"(v2), "s"(s) : "memory")
template <bool SC1> DI void dma3(unsigned m, unsigned v0, unsigned v1, unsigned v2, const char* s) {
  if constexpr (SC1) DMA3("sc1"); else DMA3("");
}
#define DMA2(POL)                                                                                         \
  asm volatile(DMA_HEAD "global_load_lds_dwordx4 %1, %3 " POL "\n\t"                                      \
               "global_load_lds_dwordx4 %2, %3 offset:1024 " POL ::"s"(m), "v"(v0), "v"(v1), "s"(s) : "memory")
template <bool SC1> DI void dma2(unsigned m, unsigned v0, unsigned v1, const char* s) {
  if constexpr (SC1) DMA2("sc1"); else DMA2("");
}
#define DMA1(POL) asm volatile(DMA_HEAD "global_load_lds_dwordx4 %1, %2 " POL ::"s"(m), "v"(v0), "s"(s) : "memory")
template <bool SC1> DI void dma1(unsigned m, unsigned v0, const char* s) {
  if constexpr (SC1) DMA1("sc1"); else DMA1("");
}

template <int OFF> DI void ldx(u32x4 (&d)[5], unsigned v, const char* s) {
  asm volatile("global_load_dwordx4 %0, %5, %6 offset:%7\n\tglobal_load_dwordx4 %1, %5, %6 offset:%8\n\tglobal_load_dwordx4 %2, %5, %6 offset:%9\n\t"
               "global_load_dwordx4 %3, %5, %6 offset:%10\n\tglobal_load_dwordx4 %4, %5, %6 offset:%11"
               : "=&v"(d[0]), "=&v"(d[1]), "=&v"(d[2]), "=&v"(d[3]), "=&v"(d[4])
               : "v"(v), "s"(s), "i"(OFF), "i"(OFF + 256), "i"(OFF + 512), "i"(OFF + 768), "i"(OFF + 1024) : "memory");
}
template <int N> DI void waitx(u32x4 (&d)[5]) { asm volatile("s_waitcnt vmcnt(%5)" : "+v"(d[0]), "+v"(d[1]), "+v"(d[2]), "+v"(d[3]), "+v"(d[4]) : "i"(N) : "memory"); }
template <int OFF> DI void tab_write(unsigned a, float r, float s) { asm volatile("ds_write_b32 %0, %1 offset:%3\n\tds_write_b32 %0, %2 offset:%4" ::"v"(a), "v"(r), "v"(s), "i"(OFF), "i"(OFF + 256) : "memory"); }
template <int CTRL> DI float dppq(float v) { return v + __int_as_float(__builtin_amdgcn_update_dpp(0, __float_as_int(v), CTRL, 0xf, 0xf, true)); }
DI float red4(float v) { return dppq<0x4E>(dppq<0xB1>(v)); }
DI void tab_read(unsigned a4, unsigned a1, f32x4 (&rs)[2], float (&r)[2], float (&s)[2]) {
  asm volatile("ds_read_b128 %0, %2\n\tds_read_b128 %1, %2 offset:16" : "=&v"(rs[0]), "=&v"(rs[1]) : "v"(a4) : "memory");
  asm volatile("ds_read_b32 %0, %4\n\tds_read_b32 %1, %4 offset:16\n\tds_read_b32 %2, %4 offset:256\n\tds_read_b32 %3, %4 offset:272"
               : "=&v"(r[0]), "=&v"(r[1]), "=&v"(s[0]), "=&v"(s[1]) : "v"(a1) : "memory");
}
DI void setm0(unsigned m) { asm volatile("s_mov_b32 m0, %0\n\ts_nop 0" ::"s"(m) : "memory"); }
template <int OFF> DI void dmai(unsigned v, const char* s) { asm volatile("global_load_lds_dwordx4 %0, %1 offset:%2" ::"v"(v), "s"(s), "i"(OFF) : "memory"); }
typedef __bf16 bf16v8 __attribute__((ext_vector_type(8)));
DI f32x4 mfc(const u32x4& a, const u32x4& b, const f32x4& c) {
  return __builtin_amdgcn_mfma_f32_16x16x32_bf16(__builtin_bit_cast(bf16v8, a), __builtin_bit_cast(bf16v8, b), c, 0, 0, 0);
}

template <int OFF> DI void gst16(const char* base, unsigned voff, const u32x4& d) {
  static_assert(OFF >= 0 && OFF < 4096, "");
  asm volatile("global_store_dwordx4 %0, %1, %2 offset:%3 sc1\n\ts_nop 1" ::"v"(voff), "v"(d), "s"(base), "i"(OFF) : "memory");
}
DI void gst4(const char* base, unsigned voff, unsigned d) {
  asm volatile("global_store_dword %0, %1, %2 sc1" ::"v"(voff), "v"(d), "s"(base) : "memory");
}
DI void gst8(const char* base, unsigned voff, const u32x2& d) {
  asm volatile("global_store_dwordx2 %0, %1, %2 sc1\n\ts_nop 1" ::"v"(voff), "v"(d), "s"(base) : "memory");
}
DI unsigned rfl(unsigned v) { return __builtin_amdgcn_readfirstlane(v); }
typedef __bf16 bf16p __attribute__((ext_vector_type(2)));
DI unsigned pk2(float lo, float hi) { return __builtin_bit_cast(unsigned, __builtin_convertvector(f32x2{lo, hi}, bf16p)); }
DI float bflo(unsigned v) { return __uint_as_float(v << 16); }
DI float bfhi(unsigned v) { return __uint_as_float(v & 0xffff0000u); }
DI f32x2 bfpair(unsigned v) { return f32x2{bflo(v), bfhi(v)}; }
template <int CTRL> DI float dppadd(float v) {
  return v + __int_as_float(__builtin_amdgcn_update_dpp(0, __float_as_int(v), CTRL, 0xf, 0xf, true));
}
DI float red8(float v) { v = dppadd<0xB1>(v); v = dppadd<0x4E>(v); return dppadd<0x141>(v); }
template <int N> DI void silu_pairs(f32x2 (&v)[N], float c, float is) {
  f32x2 t[N];
  const f32x2 c2 = {c, c}, is2 = {is, is};
#pragma unroll
  for (int k = 0; k < N; ++k) t[k] = v[k] * c2;
  sched_fence();
#pragma unroll
  for (int k = 0; k < N; ++k) { t[k][0] = __builtin_amdgcn_exp2f(t[k][0]); t[k][1] = __builtin_amdgcn_exp2f(t[k][1]); }
  sched_fence();
#pragma unroll
  for (int k = 0; k < N; ++k) t[k] = __builtin_elementwise_fma(t[k], is2, is2);
  sched_fence();
#pragma unroll
  for (int k = 0; k < N; ++k) { t[k][0] = __builtin_amdgcn_rcpf(t[k][0]); t[k][1] = __builtin_amdgcn_rcpf(t[k][1]); }
  sched_fence();
#pragma unroll
  for (int k = 0; k < N; ++k) v[k] *= t[k];
  sched_fence();
}
constexpr float NLOG2E = -1.44269504088896341f;

DI void touch(unsigned& d, const char* a) { asm volatile("global_load_dword %0, %1, off" : "+v"(d) : "v"(a) : "memory"); }
DI void touchs(unsigned& d, unsigned voff, const char* base) { asm volatile("global_load_dword %0, %1, %2" : "+v"(d) : "v"(voff), "s"(base) : "memory"); }
template <int OFF> DI void stouch(unsigned& d, const char* base) { asm volatile("s_load_dword %0, %1, %2" : "+s"(d) : "s"(base), "i"(OFF) : "memory"); }
template <int R, int C, int STRIDE> DI void stouch_blk(unsigned& d, const char* base) {
  sfor<R * C>([&](auto K) { stouch<(K.value / C) * STRIDE + (K.value % C) * 128>(d, base); });
}
template <int K0, int N> DI void stouch_c(unsigned& d, const char* base) {
  sfor<N>([&](auto K) { constexpr int k = K0 + K.value; stouch<(k & 7) * 4096 + (k >> 3) * 64>(d, base); });
}
struct Burst { int pos, first, cnt; };
constexpr int NBURST = 3;
constexpr Burst burst(int b) { return b == 0 ? Burst{9, 4, 4} : b == 1 ? Burst{17, 8, 4} : Burst{104, 12, 4}; }
constexpr int nburst(int n) { int k = 0; for (int b = 0; b < NBURST; ++b) k += burst(b).pos == n; return k; }
constexpr int scstep(int b) { return b == 0 ? 0 : b == 1 ? 2 : b == 2 ? 7 : b == 3 ? 10 : 13; }

DI void poll_req(unsigned& fv, const char* base, unsigned voff) {
  asm volatile("global_load_dword %0, %1, %2 sc1" : "+v"(fv) : "v"(voff), "s"(base) : "memory");
}
DI void poll_req4(u32x4& fv, const char* base, unsigned voff) {
  asm volatile("global_load_dwordx4 %0, %1, %2 sc1" : "+v"(fv) : "v"(voff), "s"(base) : "memory");
}
DI void poll_spin(unsigned& fv, const char* base, unsigned voff, unsigned tok) {
  asm volatile("" : "+v"(fv));
  for (int n = 0; __builtin_amdgcn_ballot_w64(fv != tok) != 0 && n < SPIN_MAX; ++n)
    asm volatile("global_load_dword %0, %1, %2 sc1\n\ts_waitcnt vmcnt(0)" : "=v"(fv) : "v"(voff), "s"(base) : "memory");
}
DI void poll_spin4(u32x4& fv, const char* base, unsigned voff, unsigned tok) {
  asm volatile("" : "+v"(fv));
  for (int n = 0; __builtin_amdgcn_ballot_w64(fv[0] != tok || fv[1] != tok || fv[2] != tok || fv[3] != tok) != 0 && n < SPIN_MAX; ++n)
    asm volatile("global_load_dwordx4 %0, %1, %2 sc1\n\ts_waitcnt vmcnt(0)" : "=v"(fv) : "v"(voff), "s"(base) : "memory");
}

#define RTW 3

struct FrB { bf16x8 a[2]; bf16x4 w[6][2]; };
struct AdX { unsigned a0, a1, a0h, a1h; };
struct AdB { unsigned e[4], r[4], r2[4]; };
template <int I, int T, int KS> DI void rdX(bf16x8& d, const AdX& ad) {
  dsr<(T & 7) * 8192 + I * 512>(d, T < 8 ? ((KS ^ I) ? ad.a1 : ad.a0) : ((KS ^ I) ? ad.a1h : ad.a0h));
}
template <int I, int U, int KS> DI void rdB(FrB& f, const AdX& ax, const AdB& ad) {
  if constexpr (I < 2) rdX<I, U, KS>(f.a[I], ax);
  else {
    constexpr int n = (I - 2) >> 1, h = (I - 2) & 1;
    constexpr unsigned base = w1slot(U, KS), in = h * 1536 + (n >> 2) * 128;
    if constexpr (base < L_R) dstr<base + in>(f.w[n][h], ad.e[n & 3]);
    else if constexpr (base - L_R < 65536) dstr<base - L_R + in>(f.w[n][h], ad.r[n & 3]);
    else dstr<base - L_R - 65536 + in>(f.w[n][h], ad.r2[n & 3]);
  }
}

struct FrA { bf16x8 a[2], b[5]; };
struct AdA { unsigned p0, p1, p0s, p1s, s0, s1; };
template <int I, int T, int KS> DI void rdA(FrA& f, const AdX& ax, const AdA& ad) {
  if constexpr (I < 2) rdX<I, T, KS>(f.a[I], ax);
  else {
    constexpr int n = I - 2, s = T & 3;
    if constexpr (n == 4) dsr<s * 20480>(f.b[4], KS ? ad.s1 : ad.s0);
    else {
      constexpr int in = (n >> 1) * 4096 + (n & 1) * 512;
      if constexpr (s < 3) dsr<s * 20480 + in>(f.b[n], KS ? ad.p1 : ad.p0);
      else dsr<in>(f.b[n], KS ? ad.p1s : ad.p0s);
    }
  }
}
template <int G, int T, int KS> DI void gapA(FrA& y, const AdX& ax, const AdA& ad) {
  if constexpr (G < 4) { rdA<2 * G, T, KS>(y, ax, ad); if constexpr (2 * G + 1 < 7) rdA<2 * G + 1, T, KS>(y, ax, ad); }
}
template <int T, int KS, bool Z, int... G> DI void halfA(f32x4 (&acc)[2][5], FrA& x, FrA& y, const AdX& ax, const AdA& ad, iseq<G...>) {
  ((mfma<Z>(acc[G / 5][G % 5], x.a[G / 5], x.b[G % 5]), gapA<G, T, KS>(y, ax, ad)), ...);
}
template <int G> DI void gapAB(FrB& xb, const AdX& ax, const AdB& ab) {
  if constexpr (G < 7) { rdB<2 * G, 0, 0>(xb, ax, ab); rdB<2 * G + 1, 0, 0>(xb, ax, ab); }
}
template <int... G> DI void halfA_last(f32x4 (&acc)[2][5], FrA& y, FrB& xb, const AdX& ax, const AdB& ab, iseq<G...>) {
  ((mfma<false>(acc[G / 5][G % 5], y.a[G / 5], y.b[G % 5]), gapAB<G>(xb, ax, ab)), ...);
}
template <int... I> DI void rdA_first(FrA& x, const AdX& ax, const AdA& ad, iseq<I...>) { (rdA<I, 0, 0>(x, ax, ad), ...); }

template <int G, int U, int KS, bool RD> DI void gapB(FrB& y, const AdX& ax, const AdB& ad) {
  if constexpr (RD && G < 7) { rdB<2 * G, U, KS>(y, ax, ad); rdB<2 * G + 1, U, KS>(y, ax, ad); }
}
DI bf16x8 cat(const bf16x4& l, const bf16x4& h) { return __builtin_shufflevector(l, h, 0, 1, 2, 3, 4, 5, 6, 7); }
template <int U, int KS, bool RD, bool Z, int... G> DI void halfB(f32x4 (&acc)[2][6], FrB& x, FrB& y, const AdX& ax, const AdB& ad, iseq<G...>) {
  ((mfma<Z>(acc[G / 6][G % 6], cat(x.w[G % 6][0], x.w[G % 6][1]), x.a[G / 6]), gapB<G, U, KS, RD>(y, ax, ad)), ...);
}

struct FrC { bf16x8 a[4], b[5]; };
struct AdC { unsigned a0, a1, b0, b1; };
template <int I, int S, int KS> DI void rdC(FrC& f, const AdC& ad) {
  if constexpr (I >= 1 && I <= 5) {
    constexpr int n = I - 1, SB = (S + BSH) & 3, off = SB * 20480 + KS * 10240 + n * 1024;
    if constexpr (SB < 3) dsr<off>(f.b[n], ad.b0); else dsr<off - 3 * 20480>(f.b[n], ad.b1);
  } else {
    constexpr int m = (I == 0) ? 0 : I - 5, off = ((S + 2) & 3) * 16384 + (m >> 1) * 4096 + (m & 1) * 512;
    dsr<off>(f.a[m], KS ? ad.a1 : ad.a0);
  }
}
template <int G, int S> DI void gapC0(FrC& y, const AdC& ad) {
  if constexpr (G < 9) rdC<G, S, 1>(y, ad);
  if constexpr (G == 4) waitl<7>();
  if constexpr (G == 9) waitl<10>();
  if constexpr (G == 14) waitl<9>();
}
template <int G, int S> DI void gapC1(FrC& x, const AdC& ad) {
  if constexpr (G < 9) rdC<G, (S + 1) & 3, 0>(x, ad);
  if constexpr (G == 19) waitl<3>();
}
template <int S, bool Z, int... G> DI void halfC0(f32x4 (&acc)[4][5], FrC& x, FrC& y, const AdC& ad, iseq<G...>) {
  ((mfma<Z>(acc[G / 5][G % 5], x.b[G % 5], x.a[G / 5]), gapC0<G, S>(y, ad)), ...);
}
template <int S, int... G> DI void halfC1(f32x4 (&acc)[4][5], FrC& x, FrC& y, const AdC& ad, iseq<G...>) {
  ((mfma<false>(acc[G / 5][G % 5], y.b[G % 5], y.a[G / 5]), gapC1<G, S>(x, ad)), ...);
}
template <int... I> DI void rdC_first(FrC& x, const AdC& ad, iseq<I...>) { (rdC<I, 0, 0>(x, ad), ...); }

template <int NN> struct FrD { bf16x8 h[2][2], w[2][NN]; };
struct AdD { unsigned a, b, bs; };
template <int NN> constexpr int nfrag(int k) { return NN == 3 ? (k == 2 ? 4 : k) : 2 + k; }
template <int NN, int I, int D, int HF> DI void rdD(FrD<NN>& f, const AdD& ad) {
  constexpr int kk = I / (NN + 2), r = I % (NN + 2), ks = HF * 2 + kk;
  if constexpr (r < 2) dsr<((D + 2) & 3) * 16384 + ks * 4096 + r * 1024>(f.h[kk][r], ad.a);
  else if constexpr (((D + BSH) & 3) < 3) dsr<((D + BSH) & 3) * 20480 + ks * 5120 + nfrag<NN>(r - 2) * 1024>(f.w[kk][r - 2], ad.b);
  else dsr<ks * 5120 + nfrag<NN>(r - 2) * 1024>(f.w[kk][r - 2], ad.bs);
}
template <int NN, int G, int D, int HF> DI void gapD(FrD<NN>& y, const AdD& ad) {
  if constexpr (2 * G < 2 * (NN + 2)) rdD<NN, 2 * G, D, HF>(y, ad);
  if constexpr (2 * G + 1 < 2 * (NN + 2)) rdD<NN, 2 * G + 1, D, HF>(y, ad);
}
template <int NN, int G, bool Z = false> DI void mfD(f32x4 (&acc)[2][NN], FrD<NN>& x) {
  constexpr int kk = G / (2 * NN), m = (G % (2 * NN)) / NN, n = G % NN;
  mfma<Z && kk == 0>(acc[m][n], x.w[kk][n], x.h[kk][m]);
}
template <int NN, int D, int HF, class F, int... G> DI void halfD(f32x4 (&acc)[2][NN], FrD<NN>& x, FrD<NN>& y, const AdD& ad, F&& f, iseq<G...>) {
  ((mfD<NN, G>(acc, x), gapD<NN, G, D, HF>(y, ad), f(IC<G>{})), ...);
}
template <int NN, bool Z, class F, int... G> DI void halfD_mf(f32x4 (&acc)[2][NN], FrD<NN>& x, F&& f, iseq<G...>) { ((mfD<NN, G, Z>(acc, x), f(IC<G>{})), ...); }
template <int OFF> DI void gld16s(u32x4& d, unsigned voff, const char* base) { asm volatile("global_load_dwordx4 %0, %1, %2 offset:%3 sc1" : "=&v"(d) : "v"(voff), "s"(base), "i"(OFF) : "memory"); }
DI void gld16(u32x4& d, unsigned voff, const char* base) { asm volatile("global_load_dwordx4 %0, %1, %2" : "=&v"(d) : "v"(voff), "s"(base) : "memory"); }
DI void gld8(u32x2& d, unsigned voff, const char* base) { asm volatile("global_load_dwordx2 %0, %1, %2" : "=&v"(d) : "v"(voff), "s"(base) : "memory"); }
template <int NN, int N> DI void wait_yp(u32x4 (&y)[NN]) {
  if constexpr (NN == 3) asm volatile("s_waitcnt vmcnt(%3)" : "+v"(y[0]), "+v"(y[1]), "+v"(y[2]) : "i"(N) : "memory");
  else asm volatile("s_waitcnt vmcnt(%2)" : "+v"(y[0]), "+v"(y[1]) : "i"(N) : "memory");
}
template <int K> DI void put_y(int own, unsigned la, unsigned yo, const char* base, const u32x4& o) {
  asm volatile("s_cmp_eq_u32 %0, 0\n\ts_cbranch_scc1 .Lyg%=\n\tds_write_b128 %1, %4 offset:%5\n\ts_branch .Lye%=\n"
               ".Lyg%=:\n\tglobal_store_dwordx4 %2, %4, %3 offset:%6 sc1\n\ts_nop 1\n.Lye%=:"
               ::"s"(own), "v"(la), "v"(yo), "s"(base), "v"(o), "i"(K * 1024), "i"((K & 3) * 1024) : "memory", "scc");
}
template <bool Z> DI void dot8(float (&t)[8], const u32x4& v, unsigned slo, unsigned shi) {
  if constexpr (Z)
    asm volatile("v_dot2_f32_bf16 %0, %8, %12, 0\n\tv_dot2_f32_bf16 %1, %8, %13, 0\n\tv_dot2_f32_bf16 %2, %9, %12, 0\n\tv_dot2_f32_bf16 %3, %9, %13, 0\n\t"
                 "v_dot2_f32_bf16 %4, %10, %12, 0\n\tv_dot2_f32_bf16 %5, %10, %13, 0\n\tv_dot2_f32_bf16 %6, %11, %12, 0\n\tv_dot2_f32_bf16 %7, %11, %13, 0"
                 : "=&v"(t[0]), "=&v"(t[1]), "=&v"(t[2]), "=&v"(t[3]), "=&v"(t[4]), "=&v"(t[5]), "=&v"(t[6]), "=&v"(t[7])
                 : "v"(v[0]), "v"(v[1]), "v"(v[2]), "v"(v[3]), "s"(slo), "s"(shi));
  else
    asm volatile("v_dot2_f32_bf16 %0, %8, %12, %0\n\tv_dot2_f32_bf16 %1, %8, %13, %1\n\tv_dot2_f32_bf16 %2, %9, %12, %2\n\tv_dot2_f32_bf16 %3, %9, %13, %3\n\t"
                 "v_dot2_f32_bf16 %4, %10, %12, %4\n\tv_dot2_f32_bf16 %5, %10, %13, %5\n\tv_dot2_f32_bf16 %6, %11, %12, %6\n\tv_dot2_f32_bf16 %7, %11, %13, %7"
                 : "+v"(t[0]), "+v"(t[1]), "+v"(t[2]), "+v"(t[3]), "+v"(t[4]), "+v"(t[5]), "+v"(t[6]), "+v"(t[7])
                 : "v"(v[0]), "v"(v[1]), "v"(v[2]), "v"(v[3]), "s"(slo), "s"(shi));
}
template <int NN, int G> DI void gapCD(FrD<NN>& x, const AdD& ad) { if constexpr (G < 2 * (NN + 2)) rdD<NN, G, 0, 0>(x, ad); }
template <int NN, int... G> DI void halfC_last(f32x4 (&acc)[4][5], FrC& y, FrD<NN>& xd, const AdD& ad, iseq<G...>) {
  ((mfma<false>(acc[G / 5][G % 5], y.b[G % 5], y.a[G / 5]), gapCD<NN, G>(xd, ad)), ...);
}

template <int NN>
DI void stage23(const Params& p, const f32x2 (&hv)[12], unsigned hoff, const AdC& adc, int cw, int lane, int blk, int wid, unsigned tok, int rb2, int cb2, int j) {
  const int wm3 = cw >> 1;
  const int tile = rb2 * 4 + cb2;
  const int i = lane & 15, q = lane >> 4, par = j & 1;
  f32x4 acc2[4][5];
  FrC xc, yc;
  int own = rfl(cw == j ? 1 : 0);
  asm volatile("" : "+s"(own));
  unsigned yl = L_OWN + lane * 16, yo[3];
#pragma unroll
  for (int k = 0; k < 3; ++k) yo[k] = WS_YP + ((tile * 4 + cw) * 4 + j) * 10240 + lane * 16 + k * 4096;
  const unsigned trow = rb2 * 128 + 64 * (j >> 1) + 32 * wm3 + 8 * (i >> 2) + (i & 3);
  const unsigned ecol = cb2 * 160 + 80 * par;
  unsigned xop = trow * 1280 + (ecol + (NN == 3 ? (par ? 16 : 0) : (par ? 48 : 32))) * 2 + q * 16;
  unsigned xos = trow * 1280 + (ecol + (par ? 0 : 64)) * 2 + q * 8;
  unsigned yhi = wm3 * 5120 + lane * 16 + L_OWN;
  unsigned yr[3], fco = WS_FC + (tile * 4 + (lane & 3)) * 4;
#pragma unroll
  for (int k = 0; k < 3; ++k) yr[k] = WS_YP + ((tile * 4 + j) * 4 + ((j + 1 + k) & 3)) * 10240 + wm3 * 5120 + 1024 + lane * 16;
  asm volatile("" : "+v"(yr[0]), "+v"(yr[1]), "+v"(yr[2]), "+v"(fco));
  AdD ad;
  ad.a = L_AR + wm3 * 2048 + lane * 16;
  ad.b = L_R + lane * 16;
  ad.bs = ad.b + 3 * 20480;
  asm volatile("" : "+v"(ad.a), "+v"(ad.b), "+v"(ad.bs), "+v"(yhi), "+v"(xop), "+v"(xos));
  asm volatile("" : "+v"(yl), "+v"(yo[0]), "+v"(yo[1]), "+v"(yo[2]));
  {

    bar();

    rdC_first(xc, adc, mkseq<9>{});
    sched_fence();
#pragma unroll
    for (int pr = 0; pr < 3; ++pr) {
      u32x4 o = {pk2(hv[4 * pr][0], hv[4 * pr][1]), pk2(hv[4 * pr + 1][0], hv[4 * pr + 1][1]), pk2(hv[4 * pr + 2][0], hv[4 * pr + 2][1]), pk2(hv[4 * pr + 3][0], hv[4 * pr + 3][1])};
      gst16<1024>(p.ws, hoff + pr * 4096, o);
    }
    waitl<0>();

    static_assert(4 > 0, "");
    auto post = [&](int g, int c) {
      waitl<0>();
      if (g == (4 >> 2) && c == (4 & 3)) waitv<0>();
      bar();
      if (g == (4 >> 2) && c == (4 & 3) && cw == 0 && lane == 0) gst4(p.ws, WS_FB + blk * 4, tok);
    };
    halfC0<0, true>(acc2, xc, yc, adc, mkseq<20>{});
    waitl<0>(); bar();
    if constexpr (1 == 0) {
#pragma unroll 1
      for (int g = 0;; ++g) {
        halfC1<0>(acc2, xc, yc, adc, mkseq<20>{});
        halfC0<1, false>(acc2, xc, yc, adc, mkseq<20>{}); post(g, 1);
        halfC1<1>(acc2, xc, yc, adc, mkseq<20>{});
        halfC0<2, false>(acc2, xc, yc, adc, mkseq<20>{}); post(g, 2);
        halfC1<2>(acc2, xc, yc, adc, mkseq<20>{});
        halfC0<3, false>(acc2, xc, yc, adc, mkseq<20>{}); post(g, 3);
        if (g == 3) break;
        halfC1<3>(acc2, xc, yc, adc, mkseq<20>{});
        halfC0<0, false>(acc2, xc, yc, adc, mkseq<20>{}); post(g + 1, 0);
      }
    } else {
#pragma unroll 1
      for (int g = 0; g < 3; ++g) {
        halfC1<0>(acc2, xc, yc, adc, mkseq<20>{});
        halfC0<1, false>(acc2, xc, yc, adc, mkseq<20>{}); post(g, 1);
        halfC1<1>(acc2, xc, yc, adc, mkseq<20>{});
        halfC0<2, false>(acc2, xc, yc, adc, mkseq<20>{}); post(g, 2);
        halfC1<2>(acc2, xc, yc, adc, mkseq<20>{});
        halfC0<3, false>(acc2, xc, yc, adc, mkseq<20>{}); post(g, 3);
        halfC1<3>(acc2, xc, yc, adc, mkseq<20>{});
        halfC0<0, false>(acc2, xc, yc, adc, mkseq<20>{}); post(g + 1, 0);
      }
      static_assert(4 <= 12, "");
      sfor<3>([&](auto CC) {
        constexpr int c = CC.value;
        halfC1<c>(acc2, xc, yc, adc, mkseq<20>{});
        halfC0<c + 1, false>(acc2, xc, yc, adc, mkseq<20>{});
        waitl<0>(); bar();
      });
    }
  }
  FrD<NN> x, y;
  halfC_last<NN>(acc2, yc, x, ad, mkseq<20>{});

  sfor<2 * (NN + 2)>([&](auto I) { rdD<NN, I.value, 0, 1>(y, ad); });
  mfma_settle();
  waitl<0>();
  bar();
  if constexpr (0 == 0) {
    auto pk = [&](auto K) {
      constexpr int pr = K.value / 5, n = K.value % 5;
      return u32x4{pk2(acc2[2 * pr][n][0], acc2[2 * pr][n][1]), pk2(acc2[2 * pr][n][2], acc2[2 * pr][n][3]),
                   pk2(acc2[2 * pr + 1][n][0], acc2[2 * pr + 1][n][1]), pk2(acc2[2 * pr + 1][n][2], acc2[2 * pr + 1][n][3])};
    };
    if (own) sfor<10>([&](auto K) { dsw<K.value * 1024>(yl, pk(K)); });
    else sfor<10>([&](auto K) { gst16<(K.value & 3) * 1024>(p.ws, yo[K.value >> 2], pk(K)); });
    waitl<0>();
  }
  auto ypiece = [&](auto HH, auto GG) {
    constexpr int h = HH.value, G = GG.value, st = 4 * NN / (0 ? 0 : 1);
    if constexpr (0 > 0 && G % st == 1 && G / st < 0) {
      constexpr int K = h * 0 + G / st;
      if constexpr (K < 10) {
        constexpr int pr = K / 5, n = K % 5;
        const u32x4 o = {pk2(acc2[2 * pr][n][0], acc2[2 * pr][n][1]), pk2(acc2[2 * pr][n][2], acc2[2 * pr][n][3]),
                         pk2(acc2[2 * pr + 1][n][0], acc2[2 * pr + 1][n][1]), pk2(acc2[2 * pr + 1][n][2], acc2[2 * pr + 1][n][3])};
        put_y<K>(own, yl, yo[K >> 2], p.ws, o);
      }
    }
  };
  f32x4 acc[2][NN];

  u32x4 xp[2];
  u32x2 xs[2];
  u32x4 yp[4][NN];
  unsigned fc = 0;
  unsigned xop1 = xop + 4 * 1280, xos1 = xos + 4 * 1280;
  asm volatile("" : "+v"(xop1), "+v"(xos1));
  auto yload = [&](auto HH, auto GG) {
    constexpr int h = HH.value - (2 * 8 + 1), G = GG.value, lph = NN == 3 ? 3 : (3 + 1) / 2 + (3 > 4), st = 4 * NN / lph;
    if constexpr (h >= 0 && G % st == 1 % st && G / st < lph) {
      constexpr int K = h * lph + G / st;
      if constexpr (K < 3 * NN) {
        constexpr int k = K / NN, n = K % NN;
        gld16s<(nfrag<NN>(n) - 1) * 1024>(yp[k][n], yr[k], p.ws);
      } else if constexpr (K == 3 * NN) gld16(xp[0], xop, p.x);
      else if constexpr (K == 3 * NN + 1) gld16(xp[1], xop1, p.x);
      else if constexpr (NN == 3 && K == 3 * NN + 2) gld8(xs[0], xos, p.x);
      else if constexpr (NN == 3 && K == 3 * NN + 3) gld8(xs[1], xos1, p.x);
    }
  };
  auto hook = [&](auto HH, auto GG) { ypiece(HH, GG); yload(HH, GG); };
  sfor<12>([&](auto DD) {
    constexpr int D = DD.value;
    if constexpr (D == 0) halfD_mf<NN, true>(acc, x, [&](auto GG) { hook(IC<0>{}, GG); }, mkseq<4 * NN>{});
    else halfD<NN, D, 1>(acc, x, y, ad, [&](auto GG) { hook(IC<2 * D>{}, GG); }, mkseq<4 * NN>{});
    waitl<0>();
    if constexpr (D == 3) waitv<0>();
    if constexpr (D == 8) {
      asm volatile("s_waitcnt vmcnt(0)" : "+v"(fc)::"memory");
      if (cw == 0) poll_spin(fc, p.ws, fco, tok);
    }

    if constexpr (D != 0 && D != 11) bar();

    if constexpr (D != 0)
    if constexpr (D == 3) { if (cw == 0 && lane == 0) gst4(p.ws, WS_FC + (tile * 4 + j) * 4, tok); }
    if constexpr (D == 7) poll_req(fc, p.ws, fco);
    if constexpr (D < 11) { halfD<NN, D + 1, 0>(acc, y, x, ad, [&](auto GG) { hook(IC<2 * D + 1>{}, GG); }, mkseq<4 * NN>{}); waitl<0>(); }
    else {
      sfor<NN>([&](auto N) { dsr4<nfrag<NN>(N.value) * 1024>(yp[3][N.value], yhi); });
      halfD_mf<NN, false>(acc, y, [&](auto GG) { hook(IC<23>{}, GG); }, mkseq<4 * NN>{});
    }
  });
  waitl<0>();

#pragma unroll
  for (int n = 0; n < NN; ++n) asm volatile("" : "+v"(yp[3][n]));
  unsigned slo[4], shi[4];
#pragma unroll
  for (int k = 0; k < 4; ++k) {
    slo[k] = ((j + 1 + k) & 2) ? 0x0000BF80u : 0x00003F80u; shi[k] = slo[k] << 16;
    asm volatile("" : "+s"(slo[k]), "+s"(shi[k]));
  }
  float ys[NN][8];
  sfor<NN>([&](auto N) { dot8<true>(ys[N.value], yp[3][N.value], slo[3], shi[3]); });
  sched_fence();
  mfma_settle();
  f32x2 o[2 * NN * 2];
#pragma unroll
  for (int m = 0; m < 2; ++m)
#pragma unroll
    for (int n = 0; n < NN; ++n) { o[(m * NN + n) * 2] = f32x2{acc[m][n][0], acc[m][n][1]}; o[(m * NN + n) * 2 + 1] = f32x2{acc[m][n][2], acc[m][n][3]}; }
  silu_pairs<2 * NN * 2>(o, NLOG2E, 1.0f);
#pragma unroll
  for (int k = 0; k < 2 * NN * 2; ++k) asm volatile("" : "+v"(o[k]));

  sfor<3>([&](auto KK) {
    constexpr int k = KK.value;
    wait_yp<NN, NN * (2 - k) + (NN == 3 ? 4 : 2)>(yp[k]);
    sfor<NN>([&](auto N) { dot8<false>(ys[N.value], yp[k][N.value], slo[k], shi[k]); });
  });
  if constexpr (NN == 3) asm volatile("s_waitcnt vmcnt(0)" : "+v"(xp[0]), "+v"(xp[1]), "+v"(xs[0]), "+v"(xs[1])::"memory");
  else asm volatile("s_waitcnt vmcnt(0)" : "+v"(xp[0]), "+v"(xp[1])::"memory");
  f32x2 xf[2][NN][2];
#pragma unroll
  for (int m = 0; m < 2; ++m)
#pragma unroll
    for (int h = 0; h < 2; ++h) {
      xf[m][0][h] = bfpair(xp[m][h]); xf[m][1][h] = bfpair(xp[m][2 + h]);
      if constexpr (NN == 3) xf[m][2][h] = bfpair(xs[m][h]);
    }
  asm volatile("s_nop 2");
  sched_fence();
#pragma unroll
  for (int m = 0; m < 2; ++m) {
    u32x4 r;
#pragma unroll
    for (int n = 0; n < 2; ++n)
#pragma unroll
      for (int h = 0; h < 2; ++h) {
        const f32x2 t = __builtin_elementwise_fma(f32x2{ys[n][4 * m + 2 * h], ys[n][4 * m + 2 * h + 1]}, o[(m * NN + n) * 2 + h], xf[m][n][h]);
        r[n * 2 + h] = pk2(t[0], t[1]);
      }
    asm volatile("global_store_dwordx4 %0, %1, %2 sc1\n\ts_nop 1" ::"v"(xop + m * 4 * 1280), "v"(r), "s"(p.out) : "memory");
    if constexpr (NN == 3) {
      u32x2 r2;
#pragma unroll
      for (int h = 0; h < 2; ++h) {
        const f32x2 t = __builtin_elementwise_fma(f32x2{ys[2][4 * m + 2 * h], ys[2][4 * m + 2 * h + 1]}, o[(m * NN + 2) * 2 + h], xf[m][2][h]);
        r2[h] = pk2(t[0], t[1]);
      }
      asm volatile("global_store_dwordx2 %0, %1, %2 sc1\n\ts_nop 1" ::"v"(xos + m * 4 * 1280), "v"(r2), "s"(p.out) : "memory");
    }
  }

}

__global__ __launch_bounds__(512, 2) void fnet_fused(Params p) {
  __shared__ __attribute__((aligned(16))) char lds[L_SIZE];
  const int tid = threadIdx.x, lane = tid & 63;
  const int wid = __builtin_amdgcn_readfirstlane(tid >> 6);
  const int blk = blockIdx.x, xcd = blk & 7, idx = blk >> 3;
  const int rb1 = (xcd & 1) * 16 + (idx >> 1), cb1 = (xcd >> 1) * 2 + (idx & 1);
  const int j = xcd & 3, rb2 = (xcd >> 2) * 8 + (idx >> 2), cb2 = idx & 3;
  asm volatile("" ::"v"((unsigned)(size_t)lds));
  u64 pc0;
  asm volatile("s_getpc_b64 %0" : "=s"(pc0));
  const char* cbase = (const char*)(pc0 & ~255ull);
  static_assert(340 <= 32 * 11, "");
  const unsigned cl0 = idx * 11 + min(wid * 2 + (lane & 1), 10);
  const unsigned clineE = min(cl0, (unsigned)(340 > 0 ? 340 - 1 : 0)) * 128;
  unsigned cdum = 0;
  if constexpr (340 > 0) touchs(cdum, clineE, cbase);

  const int lw = wid & 3;

  int leader = rfl((blk | wid) == 0 ? 1 : 0);
  asm volatile("" : "+s"(leader));
  const unsigned ho = WS_HB + ((leader ? lane : wid) & 3) * 128;
  unsigned tl = 0;
  unsigned hbk = 0;
  auto hb_post = [&]() { if (leader) { ++hbk; gst8(p.ws, WS_HB + (lane & 3) * 128, u32x2{hbk, tl}); } };
  const int pb = blk < 240 ? blk : blk - 16;

  if (wid >= 4) {
    unsigned dummy = 0;
    u32x2 hb0;
    if constexpr (1 == 0) asm volatile("global_load_dwordx2 %0, %1, %2 sc1" : "=&v"(hb0) : "v"(ho), "s"(p.ws) : "memory");
    unsigned va[5];
#pragma unroll
    for (int u = 0; u < 5; ++u) {
      const int P = lw * 5 + u, r = lane >> 3;
      const int ka = (P >> 2) == 2 ? (P & 1) << 2 : (P & 3) << 1, km = (P >> 2) == 2 ? 3 : 1;
      va[u] = (P * 8 + r) * 1280 + (((lane & 7) ^ (ka | ((r >> 1) & km))) << 4) - (u < 4 ? u * 1024 : 0);
      asm volatile("" : "+v"(va[u]));
    }
    const char* ha = (cb1 < 4 ? p.hr : p.hi) + (size_t)(cb1 & 3) * 160 * 1280;
    const char* hxb = ((xcd >> 1) < 2 ? p.hr : p.hi) + (size_t)((xcd >> 1) & 1) * 320 * 1280;
    unsigned hto;
    if constexpr (1 == 0) hto = (idx * 100 + lw * 25 + (lane < 24 ? lane : 24)) * 128;
    else { const unsigned n = idx * 60 + lw * 15 + (lane < 14 ? lane : 14), n6 = (n * 10923u) >> 16; hto = n6 * 1280 + (4 + n - n6 * 6) * 128; }
    asm volatile("" : "+v"(hto));
    unsigned sdum = 0;
    if constexpr (3 > 0) stouch_blk<5, 3, 1280>(sdum, hxb + rfl((idx * 10 + (lw >> 1) * 5) * 1280 + (lw & 1) * 64));
    if constexpr (1 == 0) touchs(dummy, hto, hxb);
    auto issA = [&](int t) {
      const unsigned m = L_R + (t & 3) * 20480 + lw * 5120;
      dma4<false>(m, va[0], va[1], va[2], va[3], ha + t * 128);
      dma1<false>(m + 4096, va[4], ha + t * 128);
    };
    bar();
    sched_fence();

    issA(0); issA(1);
    if constexpr (-1 < 0 && 1 == 0) { issA(2); issA(3); }
    sched_fence();
    unsigned vb[3];
#pragma unroll
    for (int u = 0; u < 3; ++u) {
      const int G = u * 64 + lane, row = lw * 8 + G / 24, c = G % 24;
      const int g = ((row >> 1) & 1) | (((row >> 3) & 1) << 1);
      vb[u] = row * 3072 + ((c ^ (g << 1)) << 4) - u * 1024;
      asm volatile("" : "+v"(vb[u]));
    }
    const char* w2p = p.w2 + rfl((pb / 5) * 32 * 1280 + (pb % 5) * 256);
    unsigned wt1, wt2;
    {
      const unsigned n = idx * 120 + lw * 30 + (lane < 29 ? lane : 29), n6 = (n * 43691u) >> 18;
      wt1 = n6 * 3072 + (n - n6 * 6) * 128 + (xcd >> 1) * 768;
      const int l2 = lane < 15 ? lane : 15;
      wt2 = (lw * 8 + (l2 >> 1)) * 1280 + (l2 & 1) * 128;
      asm volatile("" : "+v"(wt1), "+v"(wt2));
    }
    const char* wb = p.w1 + cb1 * 384;
    auto issW = [&](int u, int ks) { dma3<false>(w1slot(u, ks) + lw * 3072, vb[0], vb[1], vb[2], wb + (size_t)(u * 64 + ks * 32) * 3072); };
    const char* ctb = (j < 2 ? p.cr : p.ci) + (size_t)((xcd >> 2) * 1024 + idx * 32 + lw * 8) * 4096 + (j & 1) * 2048;
    auto vburst = [&](auto NNN) {
      sfor<NBURST>([&](auto BB) {
        constexpr Burst u = burst(BB.value);
        if constexpr (u.pos == NNN.value) touchs(dummy, (lane & 7) * 4096 + (u.first + min(lane >> 3, u.cnt - 1)) * 128, ctb);
      });
    };
    auto scb = [&](auto NNN) {
      sfor<5>([&](auto BB) {
        constexpr int b = BB.value, k0 = b * 15, n = 64 - k0 < 15 ? 64 - k0 : 15;
        if constexpr (scstep(b) == NNN.value) stouch_c<k0, n>(sdum, ctb);
      });
    };
    auto tch = [&](auto NNN) {
      constexpr int n = NNN.value;
      if constexpr (n == 0 && 1 == 1) touchs(dummy, hto, hxb);
      if constexpr (n == 100) touchs(dummy, wt1, p.w1);
      if constexpr (n == 100) touchs(dummy, wt2, w2p);
      vburst(NNN);
      scb(NNN);
    };
    auto step = [&](auto NNN) { tch(NNN); };
#define NT(n) (((n) == 100) + ((n) == 100) + nburst(n) + ((n) == 0 && 1 == 1))
#define XT(s, n) (((s) <= (n) - 1 ? NT(s) : 0) + ((s) + 1 <= (n) - 1 ? NT((s) + 1) : 0) + ((s) + 2 <= (n) - 1 ? NT((s) + 2) : 0) + ((s) + 3 <= (n) - 1 ? NT((s) + 3) : 0))
    if constexpr (1 == 1) tch(IC<0>{});
    if constexpr (-1 >= 0) bar();
    if constexpr (-1 >= 0 || 1 == 1) { issA(2); issA(3); }
    if constexpr (1 == 0) tch(IC<0>{});
    if constexpr (1 == 1) asm volatile("global_load_dwordx2 %0, %1, %2 sc1" : "=&v"(hb0) : "v"(ho), "s"(p.ws) : "memory");
#define NEND ((100 == 100) + (100 == 100))
    if constexpr (100 == 100) touchs(dummy, wt1, p.w1);
    if constexpr (100 == 100) touchs(dummy, wt2, w2p);
    asm volatile("s_waitcnt vmcnt(%0)" ::"i"(15 + NT(0) + (1 == 1) + NEND), "v"(cdum) : "memory");

    bar();
    step(IC<1>{});
    waitv<10 + XT(0, 2) + (1 == 1) + NEND>(); bar(); issA(4); step(IC<2>{});
    waitv<10 + (1 == 0 ? XT(0, 3) : XT(1, 3)) + (1 == 1) + NEND>(); bar(); issA(5); step(IC<3>{});
    waitv<10 + (1 == 0 ? XT(0, 4) : XT(1, 4)) + (1 == 1) + NEND>(); bar(); issA(6); step(IC<4>{});
    asm volatile("s_waitcnt vmcnt(%1)" : "+v"(hb0) : "i"(10 + XT(2, 5)) : "memory");
    const unsigned h00 = rfl(hb0[0]), h01 = rfl(hb0[1]);
    bar(); issA(7); step(IC<5>{});
    waitv<10 + XT(3, 6)>(); bar(); issA(8); step(IC<6>{});
    u32x2 hb1;
    waitv<10 + XT(4, 7)>(); bar(); issA(9);
    asm volatile("global_load_dwordx2 %0, %1, %2 sc1" : "=v"(hb1) : "v"(ho), "s"(p.ws) : "memory");
    step(IC<7>{});
    waitv<11 + XT(5, 8)>(); bar(); issW(0, 0); step(IC<8>{});
    waitv<9 + XT(6, 9)>();  bar(); issW(0, 1); issW(1, 0); step(IC<9>{});
    asm volatile("s_waitcnt vmcnt(%1)" : "+v"(hb1) : "i"(9 + XT(7, 10)) : "memory");
    for (int n = 0; rfl(hb1[0]) == h00 && rfl(hb1[1]) == h01 && n < SPIN_MAX; ++n)
      asm volatile("global_load_dwordx2 %0, %1, %2 sc1\n\ts_waitcnt vmcnt(0)" : "=v"(hb1) : "v"(ho), "s"(p.ws) : "memory");
    const unsigned tok = rfl(hb1[1]);
    bar(); issW(1, 1); step(IC<10>{});
    waitv<6 + XT(9, 11)>();  bar(); issW(2, 0); issW(2, 1); step(IC<11>{});
    unsigned vc[4];
#pragma unroll
    for (int u = 0; u < 4; ++u) {
      const int row = 8 * u + (lane >> 3), key = ((row >> 1) & 1) | (u << 1);
      vc[u] = row * 4096 + (((lane & 7) ^ key) << 4) - u * 1024;
    }
    const char* ca = (j < 2 ? p.cr : p.ci) + (size_t)(rb2 * 128 + lw * 32) * 4096 + (j & 1) * 2048;
    const unsigned vl = lane * 16;
    const char* cb = p.ws + WS_AB + (size_t)j * AB_SLICE + cb2 * 10240 + (lw >> 1) * 40960 + (lw & 1) * 5120;
    auto issCA = [&](int t) { dma4<false>(L_AR + ((t + 2) & 3) * 16384 + lw * 4096, vc[0], vc[1], vc[2], vc[3], ca + t * 128); };
    auto issCB = [&](int t) {
      const unsigned m = L_R + ((t + BSH) & 3) * 20480 + lw * 5120;
      const char* s = cb + (size_t)t * 81920;
      dma4<true>(m, vl, vl, vl, vl, s);
      dma1<true>(m + 4096, vl, s + 4096);
    };
    const unsigned fao = WS_FA + (((j >> 1) * 4 + cb2) * 32 + (j & 1) * 16 + (lane & 15)) * 4;
    unsigned fa1 = 0, fa2 = 0;
    waitv<6 + XT(10, 12)>();  bar(); issW(3, 0); issW(3, 1); step(IC<12>{});
    waitv<6 + XT(11, 13)>();  bar(); issW(4, 0); issW(4, 1); issW(5, 0);
    {
      const unsigned vw = (lw * 8 + (lane >> 4)) * 1280 + (lane & 15) * 16;
      dma2<false>((lw < 2 ? L_SP0 : L_SP1 - 4096) + lw * 2048, vw, vw + 4 * 1280 - 1024, w2p);
    }
    step(IC<13>{});
    waitv<11 + XT(12, 14)>(); bar(); issW(5, 1); issW(6, 0); issW(6, 1); step(IC<14>{});
    waitv<14 + XT(13, 15)>(); bar(); issW(7, 0); issW(7, 1); step(IC<15>{});
    waitv<12 + XT(14, 16)>(); bar(); issW(8, 0); issW(8, 1); step(IC<16>{});
    waitv<12 + XT(14, 17)>(); bar(); issW(9, 0); issW(9, 1); if constexpr (19 == 17) poll_req(fa1, p.ws, fao); step(IC<17>{});
    waitv<12 + (19 == 17) + XT(15, 18)>(); bar(); issCA(0); if constexpr (19 == 18) poll_req(fa1, p.ws, fao); step(IC<18>{});
    waitv<10 + (19 <= 18) + XT(16, 19)>(); bar(); issCA(1); if constexpr (19 == 19) poll_req(fa1, p.ws, fao); step(IC<19>{});
    waitv<9 + XT(17, 20)>(); bar();

    constexpr int FAY = NT(19) + (19 <= 18 ? 4 + NT(18) : 0) + (19 <= 17 ? 4 + NT(17) : 0);
    bool early = false;
    if constexpr (4) {
      asm volatile("s_waitcnt vmcnt(%1)" : "+v"(fa1) : "i"(FAY) : "memory");
      early = __builtin_amdgcn_ballot_w64(fa1 != tok) == 0;
    }

    if constexpr (1) {
      static_assert(!1 || (4 == 4 && 4 == 4), "");
      if (!early) {
        unsigned fs = ~tok;
        poll_spin(fs, p.ws, fao, tok);
      }

      issCB(0); issCB(1); issCB(2); issCB(3);
    } else {
      poll_req(fa2, p.ws, fao);
      if (early) { issCB(0); if constexpr (4 > 1) issCB(1); if constexpr (4 > 2) issCB(2); if constexpr (4 > 3) issCB(3); }
      step(IC<20>{});
      bar();

      if (!early) {
        if constexpr (!4) asm volatile("s_waitcnt vmcnt(%1)" : "+v"(fa1) : "i"(1 + FAY) : "memory");
        if (4 || __builtin_amdgcn_ballot_w64(fa1 != tok) != 0) {
          unsigned fs;
          asm volatile("s_waitcnt vmcnt(0)\n\tv_mov_b32 %0, %1" : "=v"(fs) : "v"(fa2) : "memory");
          poll_spin(fs, p.ws, fao, tok);
        }
        issCB(0); if constexpr (4 > 1) issCB(1); if constexpr (4 > 2) issCB(2); if constexpr (4 > 3) issCB(3);
      }
    }
    static_assert(NT(20) == 0, "");
    constexpr int NE = 4 > 1 ? 4 : 1;
    static_assert(4 >= NE, "");
    auto gsz = [](int g) constexpr { return 4 <= 2 ? ((g == 2 || g == 4) ? 4 : 5) : 4 == 3 ? ((g == 3 || g == 4) ? 4 : 5) : (g >= 4 ? 4 : 5); };
    auto grp = [&](auto GG) {
      constexpr int g = GG.value;
      if constexpr (g >= NE) {
        if constexpr (4 <= 2) {
          if constexpr (g == 1) issCB(1); else if constexpr (g == 2) issCA(2); else if constexpr (g == 3) issCB(2); else if constexpr (g == 4) issCA(3); else issCB(3);
        } else if constexpr (4 == 3) {
          if constexpr (g == 3) issCA(2); else if constexpr (g == 4) issCA(3); else issCB(3);
        } else {
          if constexpr (g == 4) issCA(2); else issCA(3);
        }
      }
    };
    sfor<4>([&](auto GG) { grp(GG); });

    waitv<(4 > 1 ? gsz(1) : 0) + (4 > 2 ? gsz(2) : 0) + (4 > 3 ? gsz(3) : 0) + (4 > 4 ? gsz(4) : 0) + (4 > 5 ? gsz(5) : 0)>();

    bar();

    sfor<6 - 4>([&](auto GG) { grp(IC<4 + GG.value>{}); });

    const char* da = p.ws + WS_H + (size_t)(rb2 >> 2) * H_GRP + ((rb2 & 3) * 2 + (j >> 1)) * 48 * 4096 + lw * 4096;
    const char* db = p.ws + WS_W2 + (size_t)(cb2 * 2 + (j & 1)) * 48 * 5120 + lw * 5120;
    auto issD = [&](int d) {
      dma4<true>(L_AR + ((d + 2) & 3) * 16384 + lw * 4096, vl, vl, vl, vl, da + (size_t)d * 16384);
      const unsigned m = L_R + ((d + BSH) & 3) * 20480 + lw * 5120;
      const char* s = db + (size_t)d * 20480;
      dma4<true>(m, vl, vl, vl, vl, s);
      dma1<true>(m + 4096, vl, s + 4096);
    };
    u32x4 fb = {0, 0, 0, 0};
    const unsigned fbo = WS_FB + lane * 16;
    sfor<16>([&](auto CC) {
      constexpr int c = CC.value;
      waitv<(4 == 4 && c == 1) ? 13 : 18>();
      if constexpr (c == 0) asm volatile("" : "+v"(fa1), "+v"(fa2));
      bar();
      if constexpr (c == 12) poll_spin4(fb, p.ws, fbo, tok);
      if constexpr (c < 12) { issCA(c + 4); issCB(c + 4); } else issD(c - 12);
      vburst(IC<100 + c>{});
      if constexpr (c == 12 - 3) poll_req4(fb, p.ws, fbo);
      if constexpr (c < 12) { } else { }
    });
    sfor<8>([&](auto DD) {
      constexpr int d = DD.value;
      waitv<18>(); bar();
      issD(d + 4);

    });
    waitv<18>(); bar();
    waitv<9>(); bar();
    waitv<0>(); bar();
    asm volatile("s_waitcnt lgkmcnt(0)" : "+v"(dummy), "+s"(sdum)::"memory");

  } else {
    const int cw = wid, wm = cw >> 1, wn = cw & 1;
    const int i = lane & 15, q = lane >> 4;
    u32x2 hb0;
    u32x4 st0, st1, st2;
    asm volatile("s_cmp_lg_u32 %3, 0\n\ts_cbranch_scc1 .Lskip_hb%=\n\tglobal_load_dwordx2 %0, %1, %2 sc1\n.Lskip_hb%=:"
                 : "=&v"(hb0) : "v"(ho), "s"(p.ws), "s"(cw) : "memory", "scc");
    asm volatile("s_cmp_eq_u32 %5, 0\n\ts_cbranch_scc1 .Lskip_st%=\n\tglobal_load_dwordx4 %0, %3, %4 sc1\n\t"
                 "global_load_dwordx4 %1, %3, %4 offset:1024 sc1\n\tglobal_load_dwordx4 %2, %3, %4 offset:2048 sc1\n.Lskip_st%=:"
                 : "=&v"(st0), "=&v"(st1), "=&v"(st2) : "v"(WS_FA + lane * 16), "s"(p.ws), "s"(leader) : "memory", "scc");
    const int la = (lane >> 2) & 3, lb = lane & 3;
    const int lrow = cw * 16 + 4 * (la & 1) + (la >> 1);
    u32x4 xr[4][5];
    unsigned xo0 = lrow * 1280 + (4 * q + lb) * 16, xo2 = xo0 + 8 * 1280;
    const char* xb_ = p.x + (size_t)rb1 * 64 * 1280;
    asm volatile("" : "+v"(xo0), "+v"(xo2));
    ldx<0>(xr[0], xo0, xb_);
    sched_fence();

    AdX ax; AdA aa; AdB ab; AdC adc;
    const int gq = lane >> 4, qq = (lane >> 2) & 3, pp = lane & 3;
    {
      const int key2 = ((i >> 1) & 1) | ((i >> 2) << 1);
      const int r0 = wm * 32 + 8 * (i >> 2) + (i & 3);
      ax.a0 = L_XN + r0 * 128 + ((q ^ key2) << 4); ax.a1 = ax.a0 ^ 64;
      ax.a0h = ax.a0 + 65536; ax.a1h = ax.a1 + 65536;
      aa.p0 = L_R + ((wn ? 96 : 0) + 8 * (i >> 2) + (i & 3)) * 128 + ((q ^ key2) << 4); aa.p1 = aa.p0 ^ 64;
      aa.p0s = aa.p0 + 3 * 20480; aa.p1s = aa.p1 + 3 * 20480;
      aa.s0 = L_R + ((wn ? 80 : 64) + i) * 128 + ((q ^ (i >> 1)) << 4); aa.s1 = aa.s0 ^ 64;
      asm volatile("" : "+v"(ax.a0), "+v"(ax.a1), "+v"(ax.a0h), "+v"(ax.a1h));
      asm volatile("" : "+v"(aa.p0), "+v"(aa.p1), "+v"(aa.p0s), "+v"(aa.p1s), "+v"(aa.s0), "+v"(aa.s1));
      sched_fence();
      ldx<2560>(xr[1], xo0, xb_);
      sched_fence();
      const int g = ((qq >> 1) & 1) | ((gq & 1) << 1);
#pragma unroll
      for (int n = 0; n < 4; ++n) {
        ab.e[n] = (8 * gq + qq) * 384 + (((6 * wn + n) ^ g) << 5) + pp * 8;
        ab.r[n] = ab.e[n] + L_R; ab.r2[n] = ab.r[n] + 65536;
        asm volatile("" : "+v"(ab.e[n]), "+v"(ab.r[n]), "+v"(ab.r2[n]));
      }
      sched_fence();
      ldx<0>(xr[2], xo2, xb_);
      sched_fence();
      const int key2c = ((i >> 1) & 1) | ((i >> 2) << 1);
      adc.a0 = L_AR + (wm * 64 + 8 * (i >> 2) + (i & 3)) * 128 + ((q ^ key2c) << 4); adc.a1 = adc.a0 ^ 64;
      adc.b0 = L_R + wn * 5120 + lane * 16; adc.b1 = adc.b0 + 3 * 20480;
      asm volatile("" : "+v"(adc.a0), "+v"(adc.a1), "+v"(adc.b0), "+v"(adc.b1));
      sched_fence();
    }
    unsigned h00 = 0, h01 = 0;
    constexpr unsigned DSB = 0x3FB5u;
    constexpr float DSC = 1.4140625f;
    float rstd[4], isd[4];
    const unsigned lt = L_SP1 + 2048;
    unsigned ltw = q == la ? lt + lrow * 4 : lt + 1024 + lane * 4;
    {
      const int lkey = (((2 * cw) & 3) << 1) ^ ((la & 1) << 2);
      unsigned lw0[4], lw1[4];
#pragma unroll
      for (int g = 0; g < 4; ++g) {
        lw0[g] = L_XN + (q >> 1) * 8192 + lrow * 128 + (((4 * (q & 1) + lb) ^ lkey ^ g) << 4);
        lw1[g] = lw0[g] + 65536;
        asm volatile("" : "+v"(lw0[g]), "+v"(lw1[g]));
      }
      asm volatile("" : "+v"(ltw));
      const unsigned one = q == (i >> 2) ? ((i & 1) ? DSB << 16 : DSB) : 0u;
      const unsigned w0 = (i & 2) ? 0u : one, w1 = (i & 2) ? one : 0u;
      u32x4 sel0 = {w0, w1, 0, 0}, sel1 = {0, 0, w0, w1}, ones = {0x3F803F80u, 0x3F803F80u, 0x3F803F80u, 0x3F803F80u};
      asm volatile("" : "+v"(sel0), "+v"(sel1), "+v"(ones));
      sched_fence();
      ldx<2560>(xr[3], xo2, xb_);
      if constexpr (-1 < 0) bar();
      sched_fence();

      asm volatile("s_waitcnt vmcnt(15)" : "+v"(hb0), "+v"(st0), "+v"(st1), "+v"(st2) : "v"(cdum) : "memory");
      waitx<15>(xr[0]);

      f32x4 sa, sb, nm[4], dg[4];
      const f32x4 z4 = {0, 0, 0, 0};
      sa = mfc(ones, xr[0][0], z4); sb = mfc(ones, xr[0][1], z4);
      sa = mfc(ones, xr[0][2], sa); sb = mfc(ones, xr[0][3], sb);
      sa = mfc(ones, xr[0][4], sa);
      h00 = rfl(hb0[0]); h01 = rfl(hb0[1]);
      if (leader) {
        unsigned c = h01 + 1;
#pragma unroll 1
        for (int n = 0; n < 1024; ++n, ++c) {
          bool hit = hb0[1] == c;
#pragma unroll
          for (int e = 0; e < 4; ++e) hit |= st0[e] == c || st1[e] == c || st2[e] == c;
          if (__builtin_amdgcn_ballot_w64(hit) == 0) break;
        }
        tl = c; hbk = h00;
        hb_post();
      }
      float mv = red4(sa[0] + sb[0]) * (-DSC / 640.0f);
      nm[0] = f32x4{mv, mv, mv, mv};
      asm volatile("" : "+v"(nm[0]));

      f32x4 d0 = mfc(sel0, xr[0][0], nm[0]), d1 = mfc(sel1, xr[0][0], nm[0]);
      u32x4 op;
      sfor<20>([&](auto SS) {
        constexpr int S = SS.value, g = S / 5, s = S % 5, gn = (S + 1) / 5, sn = (S + 1) % 5, gp = (S - 1) / 5, G = g < 3 ? g + 1 : 3;
        constexpr bool nx = g < 3;
        f32x4 e0, e1;
        u32x4 o;
        sched_fence();
        if constexpr (S == -1) bar();
        if constexpr (S == -1) bar();
        if constexpr (S < 19) e0 = mfc(sel0, xr[gn][sn], nm[gn]);
        sched_fence();
        o[0] = pk2(d0[0], d0[1]); o[1] = pk2(d0[2], d0[3]);
        sched_fence();
        if constexpr (nx && s == 3) { mv = sa[0] + sb[0]; asm volatile("" : "+v"(mv)); sched_fence(); }
        if constexpr (S < 19) e1 = mfc(sel1, xr[gn][sn], nm[gn]);
        sched_fence();
        o[2] = pk2(d1[0], d1[1]); o[3] = pk2(d1[2], d1[3]);
        sched_fence();
        if constexpr (nx && s == 3) { mv = dppq<0xB1>(mv); asm volatile("" : "+v"(mv)); sched_fence(); }
        if constexpr (S > 0) {
          if constexpr ((S - 1) % 5 == 0) dg[gp] = mfc(op, op, z4); else dg[gp] = mfc(op, op, dg[gp]);
        }
        sched_fence();
        if constexpr (nx && s == 3) { mv = dppq<0x4E>(mv); asm volatile("" : "+v"(mv)); sched_fence(); }
        if constexpr (s < 4) dsw<s * 16384 + ((g >> 1) * 8 + (g & 1) * 2) * 128>(lw0[g], o);
        else dsw<((g >> 1) * 8 + (g & 1) * 2) * 128>(lw1[g], o);
        sched_fence();
        if constexpr (nx && s == 0) {
          if constexpr (g == 0) waitx<10>(xr[1]); else if constexpr (g == 1) waitx<5>(xr[2]); else waitx<0>(xr[3]);
          sa = mfc(ones, xr[G][0], z4); sb = mfc(ones, xr[G][1], z4);
        }
        if constexpr (nx && s == 1) { sa = mfc(ones, xr[G][2], sa); sb = mfc(ones, xr[G][3], sb); }
        if constexpr (nx && s == 2) sa = mfc(ones, xr[G][4], sa);
        if constexpr (nx && s == 3) { mv *= -DSC / 640.0f; nm[G] = f32x4{mv, mv, mv, mv}; asm volatile("" : "+v"(nm[G])); }
        op = o;
        if constexpr (S < 19) { d0 = e0; d1 = e1; }
      });
      dg[3] = mfc(op, op, dg[3]);

#pragma unroll
      for (int g = 0; g < 4; ++g) {
        const float ss = red4((i & 2) ? ((i & 1) ? dg[g][3] : dg[g][2]) : ((i & 1) ? dg[g][1] : dg[g][0]));
        const float ve = ss * (1.0f / (640.0f * DSC * DSC)) + 1e-5f, r = __builtin_amdgcn_rsqf(ve);
        rstd[g] = r * (1.0f / DSC); isd[g] = ve * DSC * r;
        asm volatile("" : "+v"(rstd[g]), "+v"(isd[g]));
      }

    }

    waitl<0>();
    bar();

    hb_post();
    FrB xb, yb;
    u32x2 hb1;
    unsigned tok = tl;
    float hc[2], his[2];
    {
      f32x4 acc[2][5];
      FrA x, y;
      f32x4 rs[2];
      rdA_first(x, ax, aa, mkseq<7>{});
      waitl<0>();
      sfor<10>([&](auto TT) {
        constexpr int T = TT.value;
        halfA<T, 1, T == 0>(acc, x, y, ax, aa, mkseq<10>{});
        waitl<0>();
        bar();

        if constexpr (T == 3 || T == 6) hb_post();
        if constexpr (T == 7) {
          tab_write<0>(ltw, rstd[0], isd[0]); tab_write<8>(ltw, rstd[1], isd[1]); tab_write<32>(ltw, rstd[2], isd[2]); tab_write<40>(ltw, rstd[3], isd[3]);
        }
        if constexpr (T == 9) {
          const unsigned a4 = lt + (wm * 32 + 8 * q) * 4, a1 = lt + (wm * 32 + 8 * (i >> 2) + (i & 3)) * 4;
          tab_read(a4, a1, rs, hc, his);
        }
        if constexpr (T < 9) halfA<T + 1, 0, false>(acc, y, x, ax, aa, mkseq<10>{});
        else halfA_last(acc, y, xb, ax, ab, mkseq<10>{});
        waitl<0>();
      });
      asm volatile("" : "+v"(rs[0]), "+v"(rs[1]), "+v"(hc[0]), "+v"(hc[1]), "+v"(his[0]), "+v"(his[1]));

      asm volatile("s_cmp_lg_u32 %3, 0\n\ts_cbranch_scc1 .Lskip_h1%=\n\tglobal_load_dwordx2 %0, %1, %2 sc1\n.Lskip_h1%=:"
                   : "=&v"(hb1) : "v"(ho), "s"(p.ws), "s"(cw) : "memory", "scc");
      hc[0] *= NLOG2E; hc[1] *= NLOG2E;
      mfma_settle();
      unsigned ao = WS_AB + ((cb1 >> 2) * 2 + (rb1 >> 4)) * AB_SLICE + (((rb1 & 15) * 2 + wm) * 40 + (cb1 & 3) * 10 + wn * 5) * 1024 + lane * 16;
      sfor<5>([&](auto NN_) {
        constexpr int n = NN_.value;
        const f32x4 u = acc[0][n] * rs[0], v = acc[1][n] * rs[1];
        u32x4 o = {pk2(u[0], u[1]), pk2(u[2], u[3]), pk2(v[0], v[1]), pk2(v[2], v[3])};
        if constexpr (n == 4) { ao += 4096; asm volatile("" : "+v"(ao)); }
        gst16<(n & 3) * 1024>(p.ws, ao, o);
      });

    }
    f32x2 hv[12];
    unsigned hoff;
    {
      f32x4 acc[2][6];
      sfor<10>([&](auto UU) {
        constexpr int U = UU.value;
        halfB<U, 1, true, U == 0>(acc, xb, yb, ax, ab, mkseq<12>{});
        waitl<0>();

        if constexpr (U == 4) asm volatile("s_waitcnt vmcnt(0)" : "+v"(hb1)::"memory");

        if constexpr (U < 9 || !1) bar();

        if constexpr (U == 4) {
          if (cw == 0) {
            if (!leader) {
              for (int n = 0; rfl(hb1[0]) == h00 && rfl(hb1[1]) == h01 && n < SPIN_MAX; ++n)
                asm volatile("global_load_dwordx2 %0, %1, %2 sc1\n\ts_waitcnt vmcnt(0)" : "=v"(hb1) : "v"(ho), "s"(p.ws) : "memory");
              tok = rfl(hb1[1]);
            }
            if (lane == 0) gst4(p.ws, WS_FA + (cb1 * 32 + rb1) * 4, tok);
          }
        }
        if constexpr (U == 4) {
          const int B5 = (blk % 5) * 4 + cw, r5 = B5 % 5, dg = B5 / 5;
          const bool single = r5 == 2;
          const unsigned ta = L_SP0 + (4 * gq + qq) * 256 + cw * 64 + (single ? pp * 8 : pp * 16);
          const unsigned tb2 = ta + (single ? 32 : 8);
          bf16x4 t[2][2];
          dstr<0>(t[0][0], ta); dstr<L_SP1 - L_SP0>(t[0][1], ta); dstr<0>(t[1][0], tb2); dstr<L_SP1 - L_SP0>(t[1][1], tb2);
          asm volatile("s_waitcnt lgkmcnt(0)" : "+v"(t[0][0]), "+v"(t[0][1]), "+v"(t[1][0]), "+v"(t[1][1]));
#pragma unroll
          for (int b = 0; b < 2; ++b) {
            const int qg = 2 * dg + (r5 > 2 ? 1 : (single ? b : 0));
            const int n = single ? 4 : ((r5 == 1 || r5 == 4) ? 2 + b : b);
            const unsigned wo = (blk < 240 ? WS_W2 + ((qg * 48 + blk / 5) * 5 + n) * 1024 : WS_DUM + (cw * 2 + b) * 1024) + lane * 16;
            gst16<0>(p.ws, wo, __builtin_bit_cast(u32x4, cat(t[b][0], t[b][1])));
          }
        }
        halfB<U + 1, 0, (U < 9), false>(acc, yb, xb, ax, ab, mkseq<12>{});
        if constexpr (U < 9) waitl<0>();
      });

      u32x4 fchk = {~tl, ~tl, ~tl, ~tl};
      asm volatile("s_cmp_eq_u32 %3, 0\n\ts_cbranch_scc1 .Lskip_fa%=\n\tglobal_load_dwordx4 %0, %1, %2 sc1\n.Lskip_fa%=:"
                   : "+v"(fchk) : "v"(WS_FA + lane * 16), "s"(p.ws), "s"(leader) : "memory", "scc");
      auto lead_chk = [&]() {
        if (leader) {
          for (int n = 0; __builtin_amdgcn_ballot_w64(fchk[0] != tl || fchk[1] != tl || fchk[2] != tl || fchk[3] != tl) != 0 && n < SPIN_MAX; ++n) {
            hb_post();
            asm volatile("global_load_dwordx4 %0, %1, %2 sc1\n\ts_waitcnt vmcnt(0)" : "=v"(fchk) : "v"(WS_FA + lane * 16), "s"(p.ws) : "memory");
          }
        }
      };
      mfma_settle();
      hoff = WS_H + (rb1 >> 3) * H_GRP + (((rb1 & 7) * 48 + cb1 * 6 + wn * 3) * 4 + wm * 2) * 1024 + lane * 16;
      sfor<2>([&](auto MM) {
        constexpr int m = MM.value;
#pragma unroll
        for (int n = 0; n < 6; ++n) { hv[2 * n] = f32x2{acc[m][n][0], acc[m][n][1]}; hv[2 * n + 1] = f32x2{acc[m][n][2], acc[m][n][3]}; }
        silu_pairs<12>(hv, hc[m], his[m]);

        if constexpr (m == 0) {
#pragma unroll
          for (int pr = 0; pr < 3; ++pr) {
            u32x4 o = {pk2(hv[4 * pr][0], hv[4 * pr][1]), pk2(hv[4 * pr + 1][0], hv[4 * pr + 1][1]), pk2(hv[4 * pr + 2][0], hv[4 * pr + 2][1]), pk2(hv[4 * pr + 3][0], hv[4 * pr + 3][1])};
            gst16<0>(p.ws, hoff + pr * 4096, o);
          }

        }
      });
      asm volatile("s_waitcnt vmcnt(3)" : "+v"(fchk)::"memory");
      lead_chk();

    }
    if ((wn == 0) == (2 == 3)) stage23<2>(p, hv, hoff, adc, cw, lane, blk, wid, tok, rb2, cb2, j);
    else stage23<5 - 2>(p, hv, hoff, adc, cw, lane, blk, wid, tok, rb2, cb2, j);

  }
}

extern "C" void kernel_launch(void* const* d_in, const int* in_sizes, int n_in, void* d_out, int out_size, void* d_ws, size_t ws_size,
                              hipStream_t stream) {
  Params p;
  p.x = (const char*)d_in[0]; p.cr = (const char*)d_in[1]; p.ci = (const char*)d_in[2];
  p.hr = (const char*)d_in[3]; p.hi = (const char*)d_in[4]; p.w1 = (const char*)d_in[6]; p.w2 = (const char*)d_in[7];
  p.out = (char*)d_out; p.ws = (char*)d_ws;
  fnet_fused<<<256, 512, 0, stream>>>(p);
}
```
